```python
import torch

def _softmax(x, axis=-1):
    return torch.softmax(x, dim=axis)
def _gelu(x):
    return torch.nn.functional.gelu(x, approximate="tanh")
def _silu(x):
    return torch.nn.functional.silu(x)
def _sigmoid(x):
    return torch.sigmoid(x)
def _layernorm(x, axis=-1, eps=1e-5):
    m = x.mean(dim=axis, keepdim=True)
    v = x.var(dim=axis, keepdim=True, unbiased=False)
    return (x - m) / torch.sqrt(v + eps)
def _scatter_add(tgt, idx, src, axis=0):
    return tgt.clone().index_add_(axis, idx, src)
def _segment_sum(src, seg, n):
    out = torch.zeros((n,) + src.shape[1:], dtype=src.dtype)
    return out.index_add_(0, seg, src)
def _pad(x, pw):
    flat = tuple(v for pair in reversed(pw) for v in pair)
    return torch.nn.functional.pad(x, flat)

kernel_name = 'standalone__dcn_v2_cross__recsys'

def setup_inputs():

    _w = (lambda a: (lambda t: t.to(torch.bfloat16) if t.is_floating_point() else t)(torch.as_tensor(a)))
    B, F, D, L = 1024, 100, 64, 3
    Din = min(F * D, 1024)
    return {
        "x0": _w((lambda *s: torch.randn(*s, dtype=torch.bfloat16) * 0.05)(B, Din)),
        "ws": _w((lambda *s: torch.randn(*s, dtype=torch.bfloat16) * 0.05)(L, Din, Din)),
        "bs": _w((lambda *s: torch.randn(*s, dtype=torch.bfloat16) * 0.05)(L, Din)),
    }

def reference(x0, ws, bs):
    x = x0
    for l in range(ws.shape[0]):
        x = x0 * (x @ ws[l] + bs[l]) + x
    return x
```

```cpp
#include <hip/hip_runtime.h>
#include <hip/hip_bf16.h>
#include <stdint.h>

typedef __attribute__((ext_vector_type(8))) short bf16x8;
typedef __attribute__((ext_vector_type(4))) short bf16x4;
typedef __attribute__((ext_vector_type(16))) float f32x16;
typedef __attribute__((ext_vector_type(4))) unsigned u32x4;
typedef __attribute__((ext_vector_type(2))) unsigned u32x2;
#define LAS __attribute__((address_space(3)))
#define GAS __attribute__((address_space(1)))

constexpr int N = 1024;
constexpr int BK = 64;
constexpr int NST = 4;
constexpr int STAGE_BYTES = 16384;

__device__ __forceinline__ float bf2f(unsigned short b) { return __builtin_bit_cast(float, (unsigned)b << 16); }
__device__ __forceinline__ unsigned short f2bf(float f) { return __builtin_bit_cast(unsigned short, (__bf16)f); }
__device__ __forceinline__ float rbf(float f) { return bf2f(f2bf(f)); }

__global__ __launch_bounds__(256, 1) void layer_kernel(const unsigned short* __restrict__ x0, const unsigned short* __restrict__ xin,
                                                       const unsigned short* __restrict__ W, const unsigned short* __restrict__ bias,
                                                       unsigned short* __restrict__ out) {
  __shared__ __attribute__((aligned(16))) char smem[NST * STAGE_BYTES];
  const int tid = threadIdx.x, lane = tid & 63, wid = __builtin_amdgcn_readfirstlane(tid >> 6);
  const int b = blockIdx.x;
  const int xcd = b & 7, j = b >> 3;
  const int pm = xcd * 2 + (j >> 4), pn = j & 15;
  const int m0 = pm * 64, n0 = pn * 64;
  auto stage = [&](int kt, int buf) {
    char* sA = smem + buf * STAGE_BYTES;
    char* sW = sA + 8192;
#pragma unroll
    for (int i = 0; i < 2; ++i) {
      int r = (wid * 2 + i) * 8 + (lane >> 3);
      int c = (lane & 7) ^ (r & 7);
      __builtin_amdgcn_global_load_lds((const GAS void*)(xin + (size_t)(m0 + r) * N + kt * BK + c * 8), (LAS void*)(sA + (wid * 2 + i) * 1024), 16, 0, 0);
    }
#pragma unroll
    for (int i = 0; i < 2; ++i) {
      int r = (wid * 2 + i) * 8 + (lane >> 3);
      int c = (lane & 7) ^ (((r & 3) << 1) | ((r >> 2) & 1));
      __builtin_amdgcn_global_load_lds((const GAS void*)(W + (size_t)(kt * BK + r) * N + n0 + c * 8), (LAS void*)(sW + (wid * 2 + i) * 1024), 16, 0, 0);
    }
  };
  const int wm = wid & 1, wn = wid >> 1;
  f32x16 acc = {};
  constexpr int NT = N / BK;
  for (int s = 0; s < NST - 1; ++s) stage(s, s);
  const unsigned sbase = (unsigned)(size_t)smem;
  for (int kt = 0; kt < NT; ++kt) {
    if (kt + NST - 2 < NT) asm volatile("s_waitcnt vmcnt(8)" ::: "memory");
    else asm volatile("s_waitcnt vmcnt(0)" ::: "memory");
    __builtin_amdgcn_s_barrier();
    if (kt + NST - 1 < NT) stage(kt + NST - 1, (kt + NST - 1) % NST);
    const unsigned sA = sbase + (kt % NST) * STAGE_BYTES, sW = sA + 8192;
#pragma unroll
    for (int ks = 0; ks < 4; ++ks) {
      int r = wm * 32 + (lane & 31);
      int c = (2 * ks + (lane >> 5)) ^ (r & 7);
      bf16x8 xf = *(const LAS bf16x8*)(size_t)(sA + r * 128 + c * 16);
      bf16x4 w0, w1;
      {
        const int h = lane >> 5, blk = (lane >> 4) & 1, q = (lane & 15) >> 2, p = lane & 3;
        int rr0 = 16 * ks + 8 * h + q, rr1 = rr0 + 4;
        int ch = 4 * wn + 2 * blk + (p >> 1);
        unsigned a0 = sW + rr0 * 128 + ((ch ^ (((rr0 & 3) << 1) | ((rr0 >> 2) & 1))) * 16) + 8 * (p & 1);
        unsigned a1 = sW + rr1 * 128 + ((ch ^ (((rr1 & 3) << 1) | ((rr1 >> 2) & 1))) * 16) + 8 * (p & 1);
        w0 = __builtin_amdgcn_ds_read_tr16_b64_v4i16((LAS bf16x4*)(size_t)a0);
        w1 = __builtin_amdgcn_ds_read_tr16_b64_v4i16((LAS bf16x4*)(size_t)a1);
      }
      bf16x8 wf = __builtin_shufflevector(w0, w1, 0, 1, 2, 3, 4, 5, 6, 7);
      acc = __builtin_amdgcn_mfma_f32_32x32x16_bf16(__builtin_bit_cast(__attribute__((ext_vector_type(8))) __bf16, wf), __builtin_bit_cast(__attribute__((ext_vector_type(8))) __bf16, xf), acc, 0, 0, 0);
    }
    asm volatile("s_waitcnt lgkmcnt(0)" ::: "memory");
  }
  const int m = m0 + wm * 32 + (lane & 31);
#pragma unroll
  for (int g = 0; g < 4; ++g) {
    const int n = n0 + wn * 32 + 8 * g + 4 * (lane >> 5);
    bf16x4 vx0 = *(const bf16x4*)(x0 + (size_t)m * N + n);
    bf16x4 vx = *(const bf16x4*)(xin + (size_t)m * N + n);
    bf16x4 vb = *(const bf16x4*)(bias + n);
    bf16x4 o;
#pragma unroll
    for (int e = 0; e < 4; ++e) {
      float t = rbf(acc[4 * g + e]);
      t = rbf(t + bf2f(vb[e]));
      t = rbf(bf2f(vx0[e]) * t);
      o[e] = f2bf(t + bf2f(vx[e]));
    }
    *(bf16x4*)(out + (size_t)m * N + n) = o;
  }
}

extern "C" void kernel_launch(void* const* d_in, const int* in_sizes, int n_in, void* d_out, int out_size, void* d_ws, size_t ws_size,
                              hipStream_t stream) {
  const unsigned short* x0 = (const unsigned short*)d_in[0];
  const unsigned short* ws = (const unsigned short*)d_in[1];
  const unsigned short* bs = (const unsigned short*)d_in[2];
  unsigned short* t1 = (unsigned short*)d_ws;
  unsigned short* t2 = t1 + N * N;
  layer_kernel<<<256, 256, 0, stream>>>(x0, x0, ws, bs, t1);
  layer_kernel<<<256, 256, 0, stream>>>(x0, t1, ws + N * N, bs + N, t2);
  layer_kernel<<<256, 256, 0, stream>>>(x0, t2, ws + 2 * N * N, bs + 2 * N, (unsigned short*)d_out);
}
```

```cpp
#include <hip/hip_runtime.h>
#include <hip/hip_bf16.h>
#include <stdint.h>

typedef __attribute__((ext_vector_type(8))) short bf16x8;
typedef __attribute__((ext_vector_type(4))) short bf16x4;
typedef __attribute__((ext_vector_type(16))) float f32x16;
typedef __attribute__((ext_vector_type(4))) unsigned u32x4;
typedef __attribute__((ext_vector_type(2))) unsigned u32x2;
#define LAS __attribute__((address_space(3)))
#define GAS __attribute__((address_space(1)))

constexpr int N = 1024;
constexpr int BK = 64;
constexpr int NST = 4;
constexpr int STAGE_BYTES = 16384;

__device__ __forceinline__ float bf2f(unsigned short b) { return __builtin_bit_cast(float, (unsigned)b << 16); }
__device__ __forceinline__ unsigned short f2bf(float f) { return __builtin_bit_cast(unsigned short, (__bf16)f); }
__device__ __forceinline__ float rbf(float f) { return bf2f(f2bf(f)); }

__global__ __launch_bounds__(256, 1) void layer_kernel(const unsigned short* __restrict__ x0, const unsigned short* __restrict__ xin,
                                                       const unsigned short* __restrict__ W, const unsigned short* __restrict__ bias,
                                                       unsigned short* __restrict__ out) {
  __shared__ __attribute__((aligned(16))) char smem[NST * STAGE_BYTES];
  const int tid = threadIdx.x, lane = tid & 63, wid = __builtin_amdgcn_readfirstlane(tid >> 6);
  const int b = blockIdx.x;
  const int xcd = b & 7, j = b >> 3;
  const int pm = xcd * 2 + (j >> 4), pn = j & 15;
  const int m0 = pm * 64, n0 = pn * 64;
  auto stage = [&](int kt, int buf) {
    char* sA = smem + buf * STAGE_BYTES;
    char* sW = sA + 8192;
#pragma unroll
    for (int i = 0; i < 2; ++i) {
      int r = (wid * 2 + i) * 8 + (lane >> 3);
      int c = (lane & 7) ^ (r & 7);
      __builtin_amdgcn_global_load_lds((const GAS void*)(xin + (size_t)(m0 + r) * N + kt * BK + c * 8), (LAS void*)(sA + (wid * 2 + i) * 1024), 16, 0, 0);
    }
#pragma unroll
    for (int i = 0; i < 2; ++i) {
      int r = (wid * 2 + i) * 8 + (lane >> 3);
      int c = (lane & 7) ^ (((r & 3) << 1) | ((r >> 2) & 1));
      __builtin_amdgcn_global_load_lds((const GAS void*)(W + (size_t)(kt * BK + r) * N + n0 + c * 8), (LAS void*)(sW + (wid * 2 + i) * 1024), 16, 0, 0);
    }
  };
  const int wm = wid & 1, wn = wid >> 1;
  f32x16 acc = {};
  constexpr int NT = N / BK;
  for (int s = 0; s < NST - 1; ++s) stage(s, s);
  const unsigned sbase = (unsigned)(size_t)smem;
  for (int kt = 0; kt < NT; ++kt) {
    if (kt + NST - 2 < NT) asm volatile("s_waitcnt vmcnt(8)" ::: "memory");
    else asm volatile("s_waitcnt vmcnt(0)" ::: "memory");
    __builtin_amdgcn_s_barrier();
    if (kt + NST - 1 < NT) stage(kt + NST - 1, (kt + NST - 1) % NST);
    const unsigned sA = sbase + (kt % NST) * STAGE_BYTES, sW = sA + 8192;
#pragma unroll
    for (int ks = 0; ks < 4; ++ks) {
      int r = wm * 32 + (lane & 31);
      int c = (2 * ks + (lane >> 5)) ^ (r & 7);
      bf16x8 xf = *(const LAS bf16x8*)(size_t)(sA + r * 128 + c * 16);
      bf16x4 w0, w1;
      {
        const int h = lane >> 5, blk = (lane >> 4) & 1, q = (lane & 15) >> 2, p = lane & 3;
        int rr0 = 16 * ks + 8 * h + q, rr1 = rr0 + 4;
        int ch = 4 * wn + 2 * blk + (p >> 1);
        unsigned a0 = sW + rr0 * 128 + ((ch ^ (((rr0 & 3) << 1) | ((rr0 >> 2) & 1))) * 16) + 8 * (p & 1);
        unsigned a1 = sW + rr1 * 128 + ((ch ^ (((rr1 & 3) << 1) | ((rr1 >> 2) & 1))) * 16) + 8 * (p & 1);
        w0 = __builtin_amdgcn_ds_read_tr16_b64_v4i16((LAS bf16x4*)(size_t)a0);
        w1 = __builtin_amdgcn_ds_read_tr16_b64_v4i16((LAS bf16x4*)(size_t)a1);
      }
      bf16x8 wf = __builtin_shufflevector(w0, w1, 0, 1, 2, 3, 4, 5, 6, 7);
      acc = __builtin_amdgcn_mfma_f32_32x32x16_bf16(__builtin_bit_cast(__attribute__((ext_vector_type(8))) __bf16, wf), __builtin_bit_cast(__attribute__((ext_vector_type(8))) __bf16, xf), acc, 0, 0, 0);
    }
    asm volatile("s_waitcnt lgkmcnt(0)" ::: "memory");
  }
  const int m = m0 + wm * 32 + (lane & 31);
#pragma unroll
  for (int g = 0; g < 4; ++g) {
    const int n = n0 + wn * 32 + 8 * g + 4 * (lane >> 5);
    bf16x4 vx0 = *(const bf16x4*)(x0 + (size_t)m * N + n);
    bf16x4 vx = *(const bf16x4*)(xin + (size_t)m * N + n);
    bf16x4 vb = *(const bf16x4*)(bias + n);
    bf16x4 o;
#pragma unroll
    for (int e = 0; e < 4; ++e) {
      float t = rbf(acc[4 * g + e]);
      t = rbf(t + bf2f(vb[e]));
      t = rbf(bf2f(vx0[e]) * t);
      o[e] = f2bf(t + bf2f(vx[e]));
    }
    *(bf16x4*)(out + (size_t)m * N + n) = o;
  }
}

#define PROBE 0

#define AMP 20
__device__ __forceinline__ unsigned long long mt() { unsigned long long t; asm volatile("s_memtime %0\n\ts_waitcnt lgkmcnt(0)" : "=s"(t) :: "memory"); return t; }

__global__ __launch_bounds__(256, 1) void probe_kernel(const unsigned short* x0_, const unsigned short* W_, unsigned* sink_) {
  extern __shared__ __attribute__((aligned(16))) char dsm[];
  unsigned long long t0 = mt();
  __builtin_amdgcn_sched_barrier(0);
  unsigned long long px, pw, ps;
  {
    auto ka = __builtin_amdgcn_kernarg_segment_ptr();
    asm volatile("s_load_dwordx2 %0, %3, 0x0\n\ts_load_dwordx2 %1, %3, 0x8\n\ts_load_dwordx2 %2, %3, 0x10\n\ts_waitcnt lgkmcnt(0)" : "=&s"(px), "=&s"(pw), "=&s"(ps) : "s"(ka) : "memory");
  }
  unsigned long long t1 = mt();
  __builtin_amdgcn_sched_barrier(0);
  const unsigned short* x = (const unsigned short*)px; const unsigned short* W = (const unsigned short*)pw; unsigned* sink = (unsigned*)ps;
  const int tid = threadIdx.x, lane = tid & 63, wid = __builtin_amdgcn_readfirstlane(tid >> 6);
  const int b = blockIdx.x, xcd = b & 7, j = b >> 3;
  const int pm = xcd * 2 + (j >> 4), pn = j & 15;
  unsigned long long d = 0;
  unsigned acc = 0;
  d = 0;
  unsigned long long t2 = mt();
  while (mt() - t2 < AMP * d) __builtin_amdgcn_s_sleep(1);
  if (acc == 0x9999) sink[tid] = acc;
}

extern "C" void kernel_launch(void* const* d_in, const int* in_sizes, int n_in, void* d_out, int out_size, void* d_ws, size_t ws_size,
                              hipStream_t stream) {
  const unsigned short* x0 = (const unsigned short*)d_in[0];
  const unsigned short* ws = (const unsigned short*)d_in[1];
  const unsigned short* bs = (const unsigned short*)d_in[2];
  unsigned short* t1 = (unsigned short*)d_ws;
  unsigned short* t2 = t1 + N * N;
  hipFuncSetAttribute((const void*)probe_kernel, hipFuncAttributeMaxDynamicSharedMemorySize, 163840);
  probe_kernel<<<256, 256, 163840, stream>>>(x0, ws, (unsigned*)(t2 + N * N));
  layer_kernel<<<256, 256, 0, stream>>>(x0, x0, ws, bs, t1);
  layer_kernel<<<256, 256, 0, stream>>>(x0, t1, ws + N * N, bs + N, t2);
  layer_kernel<<<256, 256, 0, stream>>>(x0, t2, ws + 2 * N * N, bs + 2 * N, (unsigned short*)d_out);
}
```

```cpp
#include <hip/hip_runtime.h>
#include <hip/hip_bf16.h>
#include <stdint.h>

typedef __attribute__((ext_vector_type(8))) short bf16x8;
typedef __attribute__((ext_vector_type(4))) short bf16x4;
typedef __attribute__((ext_vector_type(16))) float f32x16;
typedef __attribute__((ext_vector_type(4))) unsigned u32x4;
typedef __attribute__((ext_vector_type(2))) unsigned u32x2;
#define LAS __attribute__((address_space(3)))
#define GAS __attribute__((address_space(1)))

constexpr int N = 1024;
constexpr int BK = 64;
constexpr int NST = 4;
constexpr int STAGE_BYTES = 16384;

__device__ __forceinline__ float bf2f(unsigned short b) { return __builtin_bit_cast(float, (unsigned)b << 16); }
__device__ __forceinline__ unsigned short f2bf(float f) { return __builtin_bit_cast(unsigned short, (__bf16)f); }
__device__ __forceinline__ float rbf(float f) { return bf2f(f2bf(f)); }

__global__ __launch_bounds__(256, 1) void layer_kernel(const unsigned short* __restrict__ x0, const unsigned short* __restrict__ xin,
                                                       const unsigned short* __restrict__ W, const unsigned short* __restrict__ bias,
                                                       unsigned short* __restrict__ out) {
  __shared__ __attribute__((aligned(16))) char smem[NST * STAGE_BYTES];
  const int tid = threadIdx.x, lane = tid & 63, wid = __builtin_amdgcn_readfirstlane(tid >> 6);
  const int b = blockIdx.x;
  const int xcd = b & 7, j = b >> 3;
  const int pm = xcd * 2 + (j >> 4), pn = j & 15;
  const int m0 = pm * 64, n0 = pn * 64;
  auto stage = [&](int kt, int buf) {
    char* sA = smem + buf * STAGE_BYTES;
    char* sW = sA + 8192;
#pragma unroll
    for (int i = 0; i < 2; ++i) {
      int r = (wid * 2 + i) * 8 + (lane >> 3);
      int c = (lane & 7) ^ (r & 7);
      __builtin_amdgcn_global_load_lds((const GAS void*)(xin + (size_t)(m0 + r) * N + kt * BK + c * 8), (LAS void*)(sA + (wid * 2 + i) * 1024), 16, 0, 0);
    }
#pragma unroll
    for (int i = 0; i < 2; ++i) {
      int r = (wid * 2 + i) * 8 + (lane >> 3);
      int c = (lane & 7) ^ (((r & 3) << 1) | ((r >> 2) & 1));
      __builtin_amdgcn_global_load_lds((const GAS void*)(W + (size_t)(kt * BK + r) * N + n0 + c * 8), (LAS void*)(sW + (wid * 2 + i) * 1024), 16, 0, 0);
    }
  };
  const int wm = wid & 1, wn = wid >> 1;
  f32x16 acc = {};
  constexpr int NT = N / BK;
  for (int s = 0; s < NST - 1; ++s) stage(s, s);
  const unsigned sbase = (unsigned)(size_t)smem;
  for (int kt = 0; kt < NT; ++kt) {
    if (kt + NST - 2 < NT) asm volatile("s_waitcnt vmcnt(8)" ::: "memory");
    else asm volatile("s_waitcnt vmcnt(0)" ::: "memory");
    __builtin_amdgcn_s_barrier();
    if (kt + NST - 1 < NT) stage(kt + NST - 1, (kt + NST - 1) % NST);
    const unsigned sA = sbase + (kt % NST) * STAGE_BYTES, sW = sA + 8192;
#pragma unroll
    for (int ks = 0; ks < 4; ++ks) {
      int r = wm * 32 + (lane & 31);
      int c = (2 * ks + (lane >> 5)) ^ (r & 7);
      bf16x8 xf = *(const LAS bf16x8*)(size_t)(sA + r * 128 + c * 16);
      bf16x4 w0, w1;
      {
        const int h = lane >> 5, blk = (lane >> 4) & 1, q = (lane & 15) >> 2, p = lane & 3;
        int rr0 = 16 * ks + 8 * h + q, rr1 = rr0 + 4;
        int ch = 4 * wn + 2 * blk + (p >> 1);
        unsigned a0 = sW + rr0 * 128 + ((ch ^ (((rr0 & 3) << 1) | ((rr0 >> 2) & 1))) * 16) + 8 * (p & 1);
        unsigned a1 = sW + rr1 * 128 + ((ch ^ (((rr1 & 3) << 1) | ((rr1 >> 2) & 1))) * 16) + 8 * (p & 1);
        w0 = __builtin_amdgcn_ds_read_tr16_b64_v4i16((LAS bf16x4*)(size_t)a0);
        w1 = __builtin_amdgcn_ds_read_tr16_b64_v4i16((LAS bf16x4*)(size_t)a1);
      }
      bf16x8 wf = __builtin_shufflevector(w0, w1, 0, 1, 2, 3, 4, 5, 6, 7);
      acc = __builtin_amdgcn_mfma_f32_32x32x16_bf16(__builtin_bit_cast(__attribute__((ext_vector_type(8))) __bf16, wf), __builtin_bit_cast(__attribute__((ext_vector_type(8))) __bf16, xf), acc, 0, 0, 0);
    }
    asm volatile("s_waitcnt lgkmcnt(0)" ::: "memory");
  }
  const int m = m0 + wm * 32 + (lane & 31);
#pragma unroll
  for (int g = 0; g < 4; ++g) {
    const int n = n0 + wn * 32 + 8 * g + 4 * (lane >> 5);
    bf16x4 vx0 = *(const bf16x4*)(x0 + (size_t)m * N + n);
    bf16x4 vx = *(const bf16x4*)(xin + (size_t)m * N + n);
    bf16x4 vb = *(const bf16x4*)(bias + n);
    bf16x4 o;
#pragma unroll
    for (int e = 0; e < 4; ++e) {
      float t = rbf(acc[4 * g + e]);
      t = rbf(t + bf2f(vb[e]));
      t = rbf(bf2f(vx0[e]) * t);
      o[e] = f2bf(t + bf2f(vx[e]));
    }
    *(bf16x4*)(out + (size_t)m * N + n) = o;
  }
}

#define PROBE 7
#define HOPS "15"

#define AMP 20
__device__ __forceinline__ unsigned long long mt() { unsigned long long t; asm volatile("s_memtime %0\n\ts_waitcnt lgkmcnt(0)" : "=s"(t) :: "memory"); return t; }

__global__ __launch_bounds__(256, 1) void probe_kernel(const unsigned short* x0_, const unsigned short* W_, unsigned* sink_) {
  extern __shared__ __attribute__((aligned(16))) char dsm[];
  unsigned long long t0 = mt();
  __builtin_amdgcn_sched_barrier(0);
  unsigned long long px, pw, ps;
  {
    auto ka = __builtin_amdgcn_kernarg_segment_ptr();
    asm volatile("s_load_dwordx2 %0, %3, 0x0\n\ts_load_dwordx2 %1, %3, 0x8\n\ts_load_dwordx2 %2, %3, 0x10\n\ts_waitcnt lgkmcnt(0)" : "=&s"(px), "=&s"(pw), "=&s"(ps) : "s"(ka) : "memory");
  }
  unsigned long long t1 = mt();
  __builtin_amdgcn_sched_barrier(0);
  const unsigned short* x = (const unsigned short*)px; const unsigned short* W = (const unsigned short*)pw; unsigned* sink = (unsigned*)ps;
  const int tid = threadIdx.x, lane = tid & 63, wid = __builtin_amdgcn_readfirstlane(tid >> 6);
  const int b = blockIdx.x, xcd = b & 7, j = b >> 3;
  const int pm = xcd * 2 + (j >> 4), pn = j & 15;
  unsigned long long d = 0;
  unsigned acc = 0;
  asm volatile(".rept 64\n\ts_branch " HOPS "\n\t.fill " HOPS ", 4, 0xbf800000\n\t.endr" ::: "memory");
  d = mt() - t1;
  unsigned long long t2 = mt();
  while (mt() - t2 < AMP * d) __builtin_amdgcn_s_sleep(1);
  if (acc == 0x9999) sink[tid] = acc;
}

extern "C" void kernel_launch(void* const* d_in, const int* in_sizes, int n_in, void* d_out, int out_size, void* d_ws, size_t ws_size,
                              hipStream_t stream) {
  const unsigned short* x0 = (const unsigned short*)d_in[0];
  const unsigned short* ws = (const unsigned short*)d_in[1];
  const unsigned short* bs = (const unsigned short*)d_in[2];
  unsigned short* t1 = (unsigned short*)d_ws;
  unsigned short* t2 = t1 + N * N;
  hipFuncSetAttribute((const void*)probe_kernel, hipFuncAttributeMaxDynamicSharedMemorySize, 163840);
  probe_kernel<<<256, 256, 163840, stream>>>(x0, ws, (unsigned*)(t2 + N * N));
  layer_kernel<<<256, 256, 0, stream>>>(x0, x0, ws, bs, t1);
  layer_kernel<<<256, 256, 0, stream>>>(x0, t1, ws + N * N, bs + N, t2);
  layer_kernel<<<256, 256, 0, stream>>>(x0, t2, ws + 2 * N * N, bs + 2 * N, (unsigned short*)d_out);
}
```

```cpp
#include <hip/hip_runtime.h>

__global__ __launch_bounds__(512, 2) __attribute__((aligned(1024))) void dcn_cross_kernel(const void* x0, const void* w, const void* b, void* out, void* ws) {
  __shared__ __attribute__((aligned(16))) char lds[163840];
  asm volatile("" ::"s"((unsigned)(size_t)lds));
  asm volatile(
      ".Lstart_%=:\n"
      "s_load_dwordx8 s[4:11], %0, 0\n"
      "s_load_dwordx2 s[12:13], %0, 32\n"
      "v_readfirstlane_b32 s15, %2\n"
      "v_and_b32 v1, 63, %2\n"
      "s_bfe_u32 s17, %1, 0x20001\n"
      "s_and_b32 s18, %1, 1\n"
      "s_lshr_b32 s19, %1, 6\n"
      "s_bfe_u32 s3, %1, 0x30003\n"
      "s_bfe_u32 s22, s15, 0x20006\n"
      "s_lshl2_add_u32 s20, s17, s19\n"
      "s_lshl3_add_u32 s21, s18, s3\n"
      "s_mov_b32 s47, 0\n"
      "s_bitcmp0_b32 s15, 8\n"
      "s_cbranch_scc1 .Lcompute_%=\n"
      "s_lshl_b32 s23, s22, 11\n"
      "s_mul_i32 s16, s17, 2\n"
      "s_mul_i32 s14, s18, 4\n"
      "s_add_u32 s16, s16, s14\n"
      "v_lshrrev_b32 v2, 3, v1\n"
      "v_and_b32 v3, 7, v1\n"
      "s_lshl_b32 s14, s22, 15\n"
      "s_lshl_b32 s15, s21, 7\n"
      "s_add_u32 s14, s14, s15\n"
      "v_lshl_add_u32 v5, v2, 11, s14\n"
      "v_and_b32 v4, 2, v2\n"
      "v_xor_b32 v4, v4, v3\n"
      "v_lshl_add_u32 v4, v4, 4, v5\n"
      "v_xor_b32 v5, 64, v4\n"
      "v_add_u32 v5, 15360, v5\n"
      "s_lshr_b32 s15, s3, 1\n"
      "s_lshl2_add_u32 s15, s19, s15\n"
      "s_lshl_b32 s14, s22, 5\n"
      "s_add_u32 s15, s15, s14\n"
      "v_lshrrev_b32 v8, 5, v1\n"
      "v_lshl_add_u32 v8, v8, 4, s15\n"
      "s_lshl_b32 s19, s16, 18\n"
      "s_and_b32 s16, s16, 7\n"
      "s_mov_b32 s26, 0x200000\n"
      "s_mov_b32 s27, 0x20000\n"
      "s_mov_b32 s30, 0x20000\n"
      "s_mov_b32 s31, 0x20000\n"
      "s_lshl_b32 s33, s16, 18\n"
      "s_mov_b32 s35, 0x20000\n"
      "s_mov_b32 s44, 0x1fffff\n"
      "s_lshl_b32 s17, s20, 17\n"
      "s_add_u32 m0, s23, 57344\n"
      "s_waitcnt lgkmcnt(0)\n"
      "s_add_u32 s28, s4, s17\n"
      "s_addc_u32 s29, s5, 0\n"
      "s_mov_b64 s[24:25], s[6:7]\n"
      "buffer_load_dwordx4 v4, s[28:31], 0 offen lds\n"
      "buffer_load_dwordx4 v5, s[28:31], 0 offen offset:1024 lds\n"
      "s_add_u32 m0, s23, 65536\n"
      "s_and_b32 s33, s33, s44\n"
      "buffer_load_dwordx4 v4, s[24:27], s33 offen lds\n"
      "buffer_load_dwordx4 v5, s[24:27], s33 offen offset:1024 lds\n"
      "s_add_u32 s33, s33, s35\n"
      "s_lshr_b32 s14, s6, 7\n"
      "s_sub_u32 s14, 3, s14\n"
      "s_and_b32 s14, s14, 7\n"
      "s_xor_b32 s15, s14, s3\n"
      "s_bitcmp1_b32 s15, 0\n"
      "s_cselect_b32 s15, 0, 0x8000\n"
      "s_add_u32 s14, s14, s15\n"
      "s_lshl3_add_u32 s14, s18, s14\n"
      "s_lshl_b32 s14, s14, 7\n"
      "v_lshl_add_u32 v8, v8, 11, s14\n"
      "s_lshr_b32 s14, s6, 7\n"
      "s_add_u32 s15, s14, s21\n"
      "s_and_b32 s48, s15, 3\n"
      "s_sub_u32 s14, 15, s14\n"
      "s_and_b32 s14, s14, 15\n"
      "s_xor_b32 s49, s14, s21\n"
      "s_and_b32 s49, s49, 8\n"
      "s_or_b32 s48, s48, s49\n"
      "s_cmp_eq_u32 s48, 1\n"
      "s_cbranch_scc0 .Lnot15_%=\n"
      "s_bfe_u32 s15, s15, 0x10002\n"
      "s_lshr_b32 s48, %1, 6\n"
      "s_lshl1_add_u32 s15, s48, s15\n"
      "s_lshl_b32 s48, s22, 5\n"
      "s_add_u32 s15, s15, s48\n"
      "v_lshrrev_b32 v8, 4, v1\n"
      "v_lshl_add_u32 v8, v8, 3, s15\n"
      "s_lshl_b32 s14, s14, 7\n"
      "v_lshl_add_u32 v8, v8, 11, s14\n"
      ".Lnot15_%=:\n"
      "s_and_b32 s19, s19, s44\n"
      "buffer_load_dword v12, v8, s[24:27], s19 offen\n"
      "s_add_u32 s19, s19, 0x40000\n"
      "s_and_b32 s19, s19, s44\n"
      "buffer_load_dword v12, v8, s[24:27], s19 offen\n"
      "s_add_u32 s19, s19, 0x40000\n"
      "s_add_u32 m0, s23, 73728\n"
      "s_and_b32 s33, s33, s44\n"
      "buffer_load_dwordx4 v4, s[24:27], s33 offen lds\n"
      "buffer_load_dwordx4 v5, s[24:27], s33 offen offset:1024 lds\n"
      "s_add_u32 s33, s33, s35\n"
      "s_add_u32 m0, s23, 81920\n"
      "s_and_b32 s33, s33, s44\n"
      "s_and_b32 s19, s19, s44\n"
      "buffer_load_dword v12, v8, s[24:27], s19 offen\n"
      "s_add_u32 s19, s19, 0x40000\n"
      "buffer_load_dwordx4 v4, s[24:27], s33 offen lds\n"
      "buffer_load_dwordx4 v5, s[24:27], s33 offen offset:1024 lds\n"
      "s_add_u32 s33, s33, s35\n"
      "s_lshl_b32 s14, s20, 14\n"
      "s_add_u32 s14, s14, 8454144\n"
      "s_add_u32 s40, s12, s14\n"
      "s_addc_u32 s41, s13, 0\n"
      "s_mov_b32 s43, 0x20000\n"
      "s_cmp_eq_u32 s22, 0\n"
      "s_cselect_b32 s42, 16384, 0\n"
      "v_lshlrev_b32 v9, 12, v1\n"
      "v_mov_b32 v10, 0\n"
      "buffer_atomic_or v10, v9, s[40:43], 0 offen sc0\n"
      "s_mov_b32 s42, 0\n"
      "v_mov_b32 v14, 1\n"
      "s_mov_b32 s49, 1\n"
      "s_mov_b32 s50, 2\n"
      "s_mov_b64 s[52:53], 1\n"
      "s_mov_b64 s[54:55], 0\n"
      "v_lshrrev_b32 v2, 4, v1\n"
      "s_lshl_b32 s14, s22, 3\n"
      "v_add_u32 v2, s14, v2\n"
      "v_xor_b32 v3, v2, v1\n"
      "v_and_b32 v3, 15, v3\n"
      "v_lshlrev_b32 v2, 11, v2\n"
      "v_lshl_add_u32 v6, v3, 4, v2\n"
      "v_xor_b32 v7, 64, v6\n"
      "v_add_u32 v7, 7168, v7\n"
      "s_lshl_b32 s34, s16, 8\n"
      "s_mov_b32 s45, 256\n"
      "s_mov_b32 s46, 0x7ff\n"
      "s_mov_b32 s51, 0x10000\n"
      "s_mov_b64 vcc, 0\n"
      "s_add_u32 m0, s23, 90112\n"
      "s_and_b32 s33, s33, s44\n"
      "buffer_load_dwordx4 v4, s[24:27], s33 offen lds\n"
      "buffer_load_dwordx4 v5, s[24:27], s33 offen offset:1024 lds\n"
      "s_add_u32 s33, s33, s35\n"
      "s_add_u32 m0, s23, 98304\n"
      "s_and_b32 s33, s33, s44\n"
      "s_and_b32 s19, s19, s44\n"
      "buffer_load_dword v12, v8, s[24:27], s19 offen\n"
      "s_add_u32 s19, s19, 0x40000\n"
      "buffer_load_dwordx4 v4, s[24:27], s33 offen lds\n"
      "buffer_load_dwordx4 v5, s[24:27], s33 offen offset:1024 lds\n"
      "s_add_u32 s33, s33, s35\n"
      "s_add_u32 m0, s23, 106496\n"
      "s_and_b32 s33, s33, s44\n"
      "buffer_load_dwordx4 v4, s[24:27], s33 offen lds\n"
      "buffer_load_dwordx4 v5, s[24:27], s33 offen offset:1024 lds\n"
      "s_add_u32 s33, s33, s35\n"
      "s_add_u32 m0, s23, 114688\n"
      "s_and_b32 s33, s33, s44\n"
      "s_and_b32 s19, s19, s44\n"
      "buffer_load_dword v12, v8, s[24:27], s19 offen\n"
      "s_add_u32 s19, s19, 0x40000\n"
      "buffer_load_dwordx4 v4, s[24:27], s33 offen lds\n"
      "buffer_load_dwordx4 v5, s[24:27], s33 offen offset:1024 lds\n"
      "s_add_u32 s33, s33, s35\n"
      "s_add_u32 m0, s23, 122880\n"
      "s_and_b32 s33, s33, s44\n"
      "buffer_load_dwordx4 v4, s[24:27], s33 offen lds\n"
      "buffer_load_dwordx4 v5, s[24:27], s33 offen offset:1024 lds\n"
      "s_add_u32 s33, s33, s35\n"
      ".Lld_%=:\n"
      "s_waitcnt vmcnt(18)\n"
      "s_add_u32 m0, s23, 131072\n"
      "s_and_b32 s33, s33, s44\n"
      "s_and_b32 s19, s19, s44\n"
      "buffer_load_dword v12, v8, s[24:27], s19 offen\n"
      "s_add_u32 s19, s19, 0x40000\n"
      "buffer_load_dwordx4 v4, s[24:27], s33 offen lds\n"
      "buffer_load_dwordx4 v5, s[24:27], s33 offen offset:1024 lds\n"
      "s_add_u32 s33, s33, s35\n"
      "s_waitcnt vmcnt(19)\n"
      "s_barrier\n"
      "s_add_u32 m0, s23, 139264\n"
      "s_and_b32 s33, s33, s44\n"
      "buffer_load_dwordx4 v4, s[24:27], s33 offen lds\n"
      "buffer_load_dwordx4 v5, s[24:27], s33 offen offset:1024 lds\n"
      "s_add_u32 s33, s33, s35\n"
      "s_waitcnt vmcnt(18)\n"
      "s_add_u32 m0, s23, 147456\n"
      "s_and_b32 s33, s33, s44\n"
      "s_and_b32 s19, s19, s44\n"
      "buffer_load_dword v12, v8, s[24:27], s19 offen\n"
      "s_add_u32 s19, s19, 0x40000\n"
      "buffer_load_dwordx4 v4, s[24:27], s33 offen lds\n"
      "buffer_load_dwordx4 v5, s[24:27], s33 offen offset:1024 lds\n"
      "s_add_u32 s33, s33, s35\n"
      "s_waitcnt vmcnt(18)\n"
      "s_barrier\n"
      "s_add_u32 m0, s23, 155648\n"
      "s_and_b32 s33, s33, s44\n"
      "buffer_load_dwordx4 v4, s[24:27], s33 offen lds\n"
      "buffer_load_dwordx4 v5, s[24:27], s33 offen offset:1024 lds\n"
      "s_add_u32 s33, s33, s35\n"
      "s_mov_b64 exec, s[52:53]\n"
      "v_mov_b32 v11, 0\n"
      "buffer_atomic_or v11, v9, s[40:43], 0 offen sc0\n"
      "s_mov_b64 exec, -1\n"
      "s_waitcnt vmcnt(18)\n"
      "s_add_u32 m0, s23, 65536\n"
      "s_and_b32 s33, s33, s44\n"
      "s_and_b32 s19, s19, s44\n"
      "buffer_load_dword v12, v8, s[24:27], s19 offen\n"
      "s_add_u32 s19, s19, 0x40000\n"
      "buffer_load_dwordx4 v4, s[24:27], s33 offen lds\n"
      "buffer_load_dwordx4 v5, s[24:27], s33 offen offset:1024 lds\n"
      "s_add_u32 s33, s33, s35\n"
      "s_waitcnt vmcnt(19)\n"
      "s_barrier\n"
      "s_mov_b32 exec_lo, s50\n"
      "s_mov_b32 exec_hi, 0\n"
      "buffer_atomic_add v14, v9, s[40:43], 0 offen\n"
      "s_mov_b64 exec, -1\n"
      "s_add_u32 m0, s23, 73728\n"
      "s_and_b32 s33, s33, s44\n"
      "buffer_load_dwordx4 v4, s[24:27], s33 offen lds\n"
      "buffer_load_dwordx4 v5, s[24:27], s33 offen offset:1024 lds\n"
      "s_add_u32 s33, s33, s35\n"
      "s_waitcnt vmcnt(17)\n"
      "s_barrier\n"
      "s_add_u32 m0, s23, 81920\n"
      "s_and_b32 s33, s33, s44\n"
      "buffer_load_dwordx4 v4, s[24:27], s33 offen lds\n"
      "buffer_load_dwordx4 v5, s[24:27], s33 offen offset:1024 lds\n"
      "s_add_u32 s33, s33, s35\n"
      "s_waitcnt vmcnt(14)\n"
      "s_barrier\n"
      "s_add_u32 m0, s23, 90112\n"
      "s_and_b32 s33, s33, s44\n"
      "buffer_load_dwordx4 v4, s[24:27], s33 offen lds\n"
      "buffer_load_dwordx4 v5, s[24:27], s33 offen offset:1024 lds\n"
      "s_add_u32 s33, s33, s35\n"
      "s_add_u32 s24, s24, 0x200000\n"
      "s_addc_u32 s25, s25, 0\n"
      "s_waitcnt vmcnt(10)\n"
      "s_mov_b64 exec, s[54:55]\n"
      "v_sub_u32 v11, v11, v10\n"
      "v_cmp_gt_u32 vcc, 16, v11\n"
      "s_cbranch_vccnz .Lslow0_%=\n"
      ".Lback0_%=:\n"
      "s_mov_b64 exec, -1\n"
      "s_barrier\n"
      "s_mov_b32 m0, s23\n"
      "s_and_b32 s34, s34, s46\n"
      "buffer_load_dwordx4 v6, s[28:31], s34 offen sc1 lds\n"
      "buffer_load_dwordx4 v7, s[28:31], s34 offen offset:1024 sc1 lds\n"
      "s_add_u32 s34, s34, s45\n"
      "s_waitcnt vmcnt(9)\n"
      "s_add_u32 m0, s23, 8192\n"
      "s_and_b32 s34, s34, s46\n"
      "buffer_load_dwordx4 v6, s[28:31], s34 offen sc1 lds\n"
      "buffer_load_dwordx4 v7, s[28:31], s34 offen offset:1024 sc1 lds\n"
      "s_add_u32 s34, s34, s45\n"
      "s_waitcnt vmcnt(8)\n"
      "s_barrier\n"
      "s_add_u32 m0, s23, 16384\n"
      "s_and_b32 s34, s34, s46\n"
      "buffer_load_dwordx4 v6, s[28:31], s34 offen sc1 lds\n"
      "buffer_load_dwordx4 v7, s[28:31], s34 offen offset:1024 sc1 lds\n"
      "s_add_u32 s34, s34, s45\n"
      "s_waitcnt vmcnt(8)\n"
      "s_add_u32 m0, s23, 24576\n"
      "s_and_b32 s34, s34, s46\n"
      "buffer_load_dwordx4 v6, s[28:31], s34 offen sc1 lds\n"
      "buffer_load_dwordx4 v7, s[28:31], s34 offen offset:1024 sc1 lds\n"
      "s_add_u32 s34, s34, s45\n"
      "s_lshl_b64 exec, s[52:53], 1\n"
      "v_mov_b32 v11, 0\n"
      "buffer_atomic_or v11, v9, s[40:43], 0 offen sc0\n"
      "s_mov_b64 exec, -1\n"
      "s_waitcnt vmcnt(9)\n"
      "s_barrier\n"
      "s_add_u32 m0, s23, 32768\n"
      "s_and_b32 s34, s34, s46\n"
      "buffer_load_dwordx4 v6, s[28:31], s34 offen sc1 lds\n"
      "buffer_load_dwordx4 v7, s[28:31], s34 offen offset:1024 sc1 lds\n"
      "s_add_u32 s34, s34, s45\n"
      "s_waitcnt vmcnt(9)\n"
      "s_barrier\n"
      "s_add_u32 m0, s23, 40960\n"
      "s_and_b32 s34, s34, s46\n"
      "buffer_load_dwordx4 v6, s[28:31], s34 offen sc1 lds\n"
      "buffer_load_dwordx4 v7, s[28:31], s34 offen offset:1024 sc1 lds\n"
      "s_add_u32 s34, s34, s45\n"
      "s_waitcnt vmcnt(9)\n"
      "s_barrier\n"
      "s_add_u32 m0, s23, 49152\n"
      "s_and_b32 s34, s34, s46\n"
      "buffer_load_dwordx4 v6, s[28:31], s34 offen sc1 lds\n"
      "buffer_load_dwordx4 v7, s[28:31], s34 offen offset:1024 sc1 lds\n"
      "s_add_u32 s34, s34, s45\n"
      "s_waitcnt vmcnt(9)\n"
      "s_barrier\n"
      "s_add_u32 m0, s23, 57344\n"
      "s_and_b32 s34, s34, s46\n"
      "buffer_load_dwordx4 v6, s[28:31], s34 offen sc1 lds\n"
      "buffer_load_dwordx4 v7, s[28:31], s34 offen offset:1024 sc1 lds\n"
      "s_add_u32 s34, s34, s45\n"
      "s_add_u32 s28, s28, s51\n"
      "s_addc_u32 s29, s29, 0\n"
      "s_waitcnt vmcnt(8)\n"
      "s_lshl_b64 exec, s[54:55], 1\n"
      "v_sub_u32 v11, v11, v10\n"
      "v_cmp_gt_u32 vcc, 16, v11\n"
      "s_cbranch_vccnz .Lslow1_%=\n"
      ".Lback1_%=:\n"
      "s_mov_b64 exec, -1\n"
      "s_barrier\n"
      "s_cmp_eq_u32 s22, 0\n"
      "s_cselect_b32 s42, 16384, 0\n"
      "s_mov_b32 m0, s23\n"
      "s_and_b32 s34, s34, s46\n"
      "buffer_load_dwordx4 v6, s[28:31], s34 offen sc1 lds\n"
      "buffer_load_dwordx4 v7, s[28:31], s34 offen offset:1024 sc1 lds\n"
      "s_add_u32 s34, s34, s45\n"
      "s_waitcnt vmcnt(8)\n"
      "s_barrier\n"
      "s_add_u32 m0, s23, 8192\n"
      "s_and_b32 s34, s34, s46\n"
      "buffer_load_dwordx4 v6, s[28:31], s34 offen sc1 lds\n"
      "buffer_load_dwordx4 v7, s[28:31], s34 offen offset:1024 sc1 lds\n"
      "s_add_u32 s34, s34, s45\n"
      "s_add_u32 m0, s23, 16384\n"
      "s_and_b32 s34, s34, s46\n"
      "buffer_load_dwordx4 v6, s[28:31], s34 offen sc1 lds\n"
      "buffer_load_dwordx4 v7, s[28:31], s34 offen offset:1024 sc1 lds\n"
      "s_add_u32 s34, s34, s45\n"
      "s_waitcnt vmcnt(10)\n"
      "s_barrier\n"
      "s_add_u32 m0, s23, 24576\n"
      "s_and_b32 s34, s34, s46\n"
      "buffer_load_dwordx4 v6, s[28:31], s34 offen sc1 lds\n"
      "buffer_load_dwordx4 v7, s[28:31], s34 offen offset:1024 sc1 lds\n"
      "s_add_u32 s34, s34, s45\n"
      "s_waitcnt vmcnt(10)\n"
      "s_barrier\n"
      "s_add_u32 m0, s23, 32768\n"
      "s_and_b32 s34, s34, s46\n"
      "buffer_load_dwordx4 v6, s[28:31], s34 offen sc1 lds\n"
      "buffer_load_dwordx4 v7, s[28:31], s34 offen offset:1024 sc1 lds\n"
      "s_add_u32 s34, s34, s45\n"
      "s_waitcnt vmcnt(10)\n"
      "s_barrier\n"
      "s_add_u32 m0, s23, 40960\n"
      "s_and_b32 s34, s34, s46\n"
      "buffer_load_dwordx4 v6, s[28:31], s34 offen sc1 lds\n"
      "buffer_load_dwordx4 v7, s[28:31], s34 offen offset:1024 sc1 lds\n"
      "s_add_u32 s34, s34, s45\n"
      "s_waitcnt vmcnt(10)\n"
      "s_barrier\n"
      "s_add_u32 m0, s23, 49152\n"
      "s_and_b32 s34, s34, s46\n"
      "buffer_load_dwordx4 v6, s[28:31], s34 offen sc1 lds\n"
      "buffer_load_dwordx4 v7, s[28:31], s34 offen offset:1024 sc1 lds\n"
      "s_add_u32 s34, s34, s45\n"
      "s_waitcnt vmcnt(10)\n"
      "s_barrier\n"
      "s_add_u32 m0, s23, 57344\n"
      "s_and_b32 s34, s34, s46\n"
      "buffer_load_dwordx4 v6, s[28:31], s34 offen sc1 lds\n"
      "buffer_load_dwordx4 v7, s[28:31], s34 offen offset:1024 sc1 lds\n"
      "s_add_u32 s34, s34, s45\n"
      "s_add_u32 s28, s28, 4063232\n"
      "s_addc_u32 s29, s29, 0\n"
      "s_cmp_eq_u32 s47, 2\n"
      "s_cbranch_scc1 .Lldtail_%=\n"
      "s_waitcnt vmcnt(10)\n"
      "s_barrier\n"
      "s_add_u32 m0, s23, 65536\n"
      "s_and_b32 s33, s33, s44\n"
      "s_and_b32 s19, s19, s44\n"
      "buffer_load_dword v12, v8, s[24:27], s19 offen\n"
      "s_add_u32 s19, s19, 0x40000\n"
      "s_and_b32 s19, s19, s44\n"
      "buffer_load_dword v12, v8, s[24:27], s19 offen\n"
      "s_add_u32 s19, s19, 0x40000\n"
      "buffer_load_dwordx4 v4, s[24:27], s33 offen lds\n"
      "buffer_load_dwordx4 v5, s[24:27], s33 offen offset:1024 lds\n"
      "s_add_u32 s33, s33, s35\n"
      "s_add_u32 m0, s23, 73728\n"
      "s_and_b32 s33, s33, s44\n"
      "buffer_load_dwordx4 v4, s[24:27], s33 offen lds\n"
      "buffer_load_dwordx4 v5, s[24:27], s33 offen offset:1024 lds\n"
      "s_add_u32 s33, s33, s35\n"
      "s_waitcnt vmcnt(14)\n"
      "s_barrier\n"
      "s_add_u32 m0, s23, 81920\n"
      "s_and_b32 s33, s33, s44\n"
      "s_and_b32 s19, s19, s44\n"
      "buffer_load_dword v12, v8, s[24:27], s19 offen\n"
      "s_add_u32 s19, s19, 0x40000\n"
      "buffer_load_dwordx4 v4, s[24:27], s33 offen lds\n"
      "buffer_load_dwordx4 v5, s[24:27], s33 offen offset:1024 lds\n"
      "s_add_u32 s33, s33, s35\n"
      "s_waitcnt vmcnt(15)\n"
      "s_barrier\n"
      "s_add_u32 m0, s23, 90112\n"
      "s_and_b32 s33, s33, s44\n"
      "buffer_load_dwordx4 v4, s[24:27], s33 offen lds\n"
      "buffer_load_dwordx4 v5, s[24:27], s33 offen offset:1024 lds\n"
      "s_add_u32 s33, s33, s35\n"
      "s_waitcnt vmcnt(15)\n"
      "s_barrier\n"
      "s_add_u32 m0, s23, 98304\n"
      "s_and_b32 s33, s33, s44\n"
      "s_and_b32 s19, s19, s44\n"
      "buffer_load_dword v12, v8, s[24:27], s19 offen\n"
      "s_add_u32 s19, s19, 0x40000\n"
      "buffer_load_dwordx4 v4, s[24:27], s33 offen lds\n"
      "buffer_load_dwordx4 v5, s[24:27], s33 offen offset:1024 lds\n"
      "s_add_u32 s33, s33, s35\n"
      "s_add_u32 m0, s23, 106496\n"
      "s_and_b32 s33, s33, s44\n"
      "buffer_load_dwordx4 v4, s[24:27], s33 offen lds\n"
      "buffer_load_dwordx4 v5, s[24:27], s33 offen offset:1024 lds\n"
      "s_add_u32 s33, s33, s35\n"
      "s_waitcnt vmcnt(18)\n"
      "s_barrier\n"
      "s_mov_b32 exec_lo, s49\n"
      "s_mov_b32 exec_hi, 0\n"
      "buffer_atomic_add v14, v9, s[40:43], 0 offen\n"
      "s_mov_b64 exec, -1\n"
      "s_add_u32 m0, s23, 114688\n"
      "s_and_b32 s33, s33, s44\n"
      "s_and_b32 s19, s19, s44\n"
      "buffer_load_dword v12, v8, s[24:27], s19 offen\n"
      "s_add_u32 s19, s19, 0x40000\n"
      "buffer_load_dwordx4 v4, s[24:27], s33 offen lds\n"
      "buffer_load_dwordx4 v5, s[24:27], s33 offen offset:1024 lds\n"
      "s_add_u32 s33, s33, s35\n"
      "s_waitcnt vmcnt(20)\n"
      "s_barrier\n"
      "s_add_u32 m0, s23, 122880\n"
      "s_and_b32 s33, s33, s44\n"
      "buffer_load_dwordx4 v4, s[24:27], s33 offen lds\n"
      "buffer_load_dwordx4 v5, s[24:27], s33 offen offset:1024 lds\n"
      "s_add_u32 s33, s33, s35\n"
      "s_add_u32 s47, s47, 1\n"
      "s_cmp_eq_u32 s47, 1\n"
      "s_cbranch_scc1 .Lldfirst_%=\n"
      "s_lshl_b64 s[52:53], s[52:53], 2\n"
      "s_lshl_b64 s[54:55], s[54:55], 2\n"
      "s_mov_b32 s50, 8\n"
      "s_branch .Lld_%=\n"
      ".Lldfirst_%=:\n"
      "s_cmp_eq_u32 s22, 0\n"
      "s_cselect_b32 s54, 1, 0\n"
      "s_mov_b32 s49, 7\n"
      "s_add_u32 s28, s12, 2047\n"
      "s_addc_u32 s29, s13, 0\n"
      "s_and_b32 s28, s28, 0xfffff800\n"
      "s_lshl_b32 s15, s20, 18\n"
      "s_add_u32 s28, s28, s15\n"
      "s_addc_u32 s29, s29, 0\n"
      "s_lshl_b32 s34, s16, 14\n"
      "s_mov_b32 s45, 16384\n"
      "s_mov_b32 s46, 131071\n"
      "s_mov_b32 s51, 131072\n"
      "v_lshlrev_b32 v6, 4, v1\n"
      "v_cmp_lt_u32 vcc, 23, v1\n"
      "v_add_u32 v7, 128, v6\n"
      "s_lshl_b32 s14, s23, 1\n"
      "v_cndmask_b32 v6, v6, v7, vcc\n"
      "v_add_u32 v6, s14, v6\n"
      "v_add_u32 v7, 1024, v6\n"
      "s_mov_b64 vcc, 0\n"
      "s_branch .Lld_%=\n"
      ".Lslow0_%=:\n"
      "s_mov_b32 s48, 0x100000\n"
      ".Lslowl0_%=:\n"
      "v_mov_b32 v11, 0\n"
      "buffer_atomic_or v11, v9, s[40:43], 0 offen sc0\n"
      "s_sub_u32 s48, s48, 1\n"
      "s_waitcnt vmcnt(0)\n"
      "v_sub_u32 v11, v11, v10\n"
      "v_cmp_gt_u32 vcc, 16, v11\n"
      "s_cselect_b64 vcc, 0, vcc\n"
      "s_cbranch_vccnz .Lslowl0_%=\n"
      "s_branch .Lback0_%=\n"
      ".Lslow1_%=:\n"
      "s_mov_b32 s48, 0x100000\n"
      ".Lslowl1_%=:\n"
      "v_mov_b32 v11, 0\n"
      "buffer_atomic_or v11, v9, s[40:43], 0 offen sc0\n"
      "s_sub_u32 s48, s48, 1\n"
      "s_waitcnt vmcnt(0)\n"
      "v_sub_u32 v11, v11, v10\n"
      "v_cmp_gt_u32 vcc, 16, v11\n"
      "s_cselect_b64 vcc, 0, vcc\n"
      "s_cbranch_vccnz .Lslowl1_%=\n"
      "s_branch .Lback1_%=\n"
      ".Lldtail_%=:\n"
      "s_waitcnt vmcnt(10)\n"
      "s_barrier\n"
      "s_waitcnt vmcnt(8)\n"
      "s_barrier\n"
      "s_waitcnt vmcnt(6)\n"
      "s_barrier\n"
      "s_waitcnt vmcnt(4)\n"
      "s_barrier\n"
      "s_waitcnt vmcnt(2)\n"
      "s_barrier\n"
      "s_waitcnt vmcnt(0)\n"
      "s_barrier\n"
      "s_branch .Lend_%=\n"
      ".Lcompute_%=:\n"
      "v_mov_b32 v102, v1\n"
      "s_lshl_b32 s17, s20, 17\n"
      "v_lshrrev_b32 v72, 4, v102\n"
      "v_and_b32 v73, 15, v102\n"
      "s_getpc_b64 s[24:25]\n"
      ".Lpc_%=:\n"
      "s_add_u32 s24, s24, .Lstart_%=-.Lpc_%=\n"
      "s_addc_u32 s25, s25, -1\n"
      "s_lshr_b32 s14, %1, 3\n"
      "s_lshl2_add_u32 s14, s14, s22\n"
      "s_and_b32 s14, s14, 63\n"
      "s_min_u32 s14, s14, (.Lend_%=-.Lstart_%=)/128\n"
      "s_lshl_b32 s14, s14, 7\n"
      "v_mov_b32 v104, s14\n"
      "global_load_dword v105, v104, s[24:25]\n"
      "s_lshl2_add_u32 s14, s21, s22\n"
      "s_lshl_b32 s14, s14, 5\n"
      "v_lshl_add_u32 v100, v72, 3, s14\n"
      "v_add_u32 v101, 0x1000, v100\n"
      "s_waitcnt lgkmcnt(0)\n"
      "global_load_dwordx2 v[12:13], v100, s[8:9] offset:0\n"
      "global_load_dwordx2 v[8:9], v100, s[8:9] offset:2048\n"
      "global_load_dwordx2 v[10:11], v101, s[8:9] offset:0\n"
      "s_mov_b32 s18, 0xffff0000\n"
      "s_mov_b32 s51, 131072\n"
      "s_lshr_b32 s14, s21, 1\n"
      "s_lshl_b32 s14, s14, 2\n"
      "s_and_b32 s15, s21, 1\n"
      "s_add_u32 s14, s14, s15\n"
      "s_lshl_b32 s14, s14, 12\n"
      "s_add_u32 s36, s12, 2047\n"
      "s_addc_u32 s37, s13, 0\n"
      "s_and_b32 s36, s36, 0xfffff800\n"
      "s_lshl_b32 s15, s20, 18\n"
      "s_add_u32 s15, s15, s14\n"
      "s_add_u32 s36, s36, s15\n"
      "s_addc_u32 s37, s37, 0\n"
      "s_mov_b32 s38, 262144\n"
      "s_mov_b32 s39, 0x20000\n"
      "v_lshrrev_b32 v101, 1, v72\n"
      "s_and_b32 s14, s22, 1\n"
      "s_lshl_b32 s14, s14, 9\n"
      "v_lshl_add_u32 v7, v73, 4, s14\n"
      "v_lshl_add_u32 v7, v101, 8, v7\n"
      "v_add_u32 v101, 128, v7\n"
      "v_cmp_lt_u32 vcc, 383, v7\n"
      "s_nop 1\n"
      "v_cndmask_b32 v7, v7, v101, vcc\n"
      "v_and_b32 v100, 1, v72\n"
      "v_lshl_add_u32 v7, v100, 13, v7\n"
      "s_lshr_b32 s14, s22, 1\n"
      "s_lshl_b32 s14, s14, 11\n"
      "v_add_u32 v7, s14, v7\n"
      "v_xor_b32 v101, v72, v73\n"
      "v_lshlrev_b32 v4, 8, v73\n"
      "v_lshl_add_u32 v1, v101, 4, v4\n"
      "v_xor_b32 v101, 4, v101\n"
      "v_lshl_add_u32 v2, v101, 4, v4\n"
      "v_xor_b32 v101, 12, v101\n"
      "v_lshl_add_u32 v3, v101, 4, v4\n"
      "v_xor_b32 v101, 4, v101\n"
      "v_lshl_add_u32 v4, v101, 4, v4\n"
      "v_bfe_u32 v100, v73, 1, 1\n"
      "v_bfe_u32 v101, v73, 3, 1\n"
      "v_lshl_or_b32 v100, v101, 1, v100\n"
      "v_xor_b32 v100, s22, v100\n"
      "v_lshlrev_b32 v101, 7, v73\n"
      "v_lshl_add_u32 v101, v72, 3, v101\n"
      "v_lshl_add_u32 v64, v100, 5, v101\n"
      "v_bfe_u32 v100, v102, 2, 2\n"
      "v_lshl_add_u32 v100, v72, 3, v100\n"
      "v_lshlrev_b32 v100, 7, v100\n"
      "v_and_b32 v101, 3, v102\n"
      "v_lshl_add_u32 v100, v101, 3, v100\n"
      "v_bfe_u32 v101, v102, 3, 2\n"
      "v_xor_b32 v101, s22, v101\n"
      "v_lshl_add_u32 v100, v101, 5, v100\n"
      "v_add_u32 v5, 65536, v100\n"
      "v_add_u32 v6, 131072, v100\n"
      "s_barrier\n"
      "ds_read_b64 v[80:81], v64 offset:57344\n"
      "ds_read_b64 v[82:83], v64 offset:59392\n"
      "ds_read_b64 v[84:85], v64 offset:61440\n"
      "ds_read_b64 v[86:87], v64 offset:63488\n"
      "s_waitcnt vmcnt(0) lgkmcnt(0)\n"
      "v_lshlrev_b32 v48, 16, v80\n"
      "v_and_b32 v49, s18, v80\n"
      "v_lshlrev_b32 v50, 16, v81\n"
      "v_and_b32 v51, s18, v81\n"
      "v_lshlrev_b32 v52, 16, v82\n"
      "v_and_b32 v53, s18, v82\n"
      "v_lshlrev_b32 v54, 16, v83\n"
      "v_and_b32 v55, s18, v83\n"
      "v_mov_b32 v32, v48\n"
      "v_mov_b32 v33, v49\n"
      "v_mov_b32 v34, v50\n"
      "v_mov_b32 v35, v51\n"
      "v_mov_b32 v36, v52\n"
      "v_mov_b32 v37, v53\n"
      "v_mov_b32 v38, v54\n"
      "v_mov_b32 v39, v55\n"
      "v_lshlrev_b32 v56, 16, v84\n"
      "v_and_b32 v57, s18, v84\n"
      "v_lshlrev_b32 v58, 16, v85\n"
      "v_and_b32 v59, s18, v85\n"
      "v_lshlrev_b32 v60, 16, v86\n"
      "v_and_b32 v61, s18, v86\n"
      "v_lshlrev_b32 v62, 16, v87\n"
      "v_and_b32 v63, s18, v87\n"
      "v_mov_b32 v40, v56\n"
      "v_mov_b32 v41, v57\n"
      "v_mov_b32 v42, v58\n"
      "v_mov_b32 v43, v59\n"
      "v_mov_b32 v44, v60\n"
      "v_mov_b32 v45, v61\n"
      "v_mov_b32 v46, v62\n"
      "v_mov_b32 v47, v63\n"
      "s_branch .Lcp1_%=\n"
      ".Lcp_%=:\n"
      "s_barrier\n"
      ".Lcp1_%=:\n"
      "ds_read_b64_tr_b16 a[0:1], v5 offset:0\n"
      "ds_read_b64_tr_b16 a[2:3], v5 offset:512\n"
      "ds_read_b64_tr_b16 a[4:5], v5 offset:4096\n"
      "ds_read_b64_tr_b16 a[6:7], v5 offset:4608\n"
      "ds_read_b64_tr_b16 a[8:9], v5 offset:8192\n"
      "ds_read_b64_tr_b16 a[10:11], v5 offset:8704\n"
      "ds_read_b64_tr_b16 a[12:13], v5 offset:12288\n"
      "ds_read_b64_tr_b16 a[14:15], v5 offset:12800\n"
      "s_waitcnt lgkmcnt(0)\n"
      "s_barrier\n"
      "ds_read_b64_tr_b16 a[16:17], v5 offset:16384\n"
      "ds_read_b64_tr_b16 a[18:19], v5 offset:16896\n"
      "ds_read_b64_tr_b16 a[20:21], v5 offset:20480\n"
      "ds_read_b64_tr_b16 a[22:23], v5 offset:20992\n"
      "ds_read_b64_tr_b16 a[24:25], v5 offset:24576\n"
      "ds_read_b64_tr_b16 a[26:27], v5 offset:25088\n"
      "ds_read_b64_tr_b16 a[28:29], v5 offset:28672\n"
      "ds_read_b64_tr_b16 a[30:31], v5 offset:29184\n"
      "s_waitcnt lgkmcnt(0) vmcnt(0)\n"
      "s_barrier\n"
      "ds_read_b64_tr_b16 a[32:33], v5 offset:32768\n"
      "ds_read_b64_tr_b16 a[34:35], v5 offset:33280\n"
      "ds_read_b64_tr_b16 a[36:37], v5 offset:36864\n"
      "ds_read_b64_tr_b16 a[38:39], v5 offset:37376\n"
      "ds_read_b64_tr_b16 a[40:41], v5 offset:40960\n"
      "ds_read_b64_tr_b16 a[42:43], v5 offset:41472\n"
      "ds_read_b64_tr_b16 a[44:45], v5 offset:45056\n"
      "ds_read_b64_tr_b16 a[46:47], v5 offset:45568\n"
      "s_waitcnt lgkmcnt(0)\n"
      "s_barrier\n"
      "ds_read_b64_tr_b16 a[48:49], v5 offset:49152\n"
      "ds_read_b64_tr_b16 a[50:51], v5 offset:49664\n"
      "ds_read_b64_tr_b16 a[52:53], v5 offset:53248\n"
      "ds_read_b64_tr_b16 a[54:55], v5 offset:53760\n"
      "ds_read_b64_tr_b16 a[56:57], v5 offset:57344\n"
      "ds_read_b64_tr_b16 a[58:59], v5 offset:57856\n"
      "ds_read_b64_tr_b16 a[60:61], v5 offset:61440\n"
      "ds_read_b64_tr_b16 a[62:63], v5 offset:61952\n"
      "s_waitcnt lgkmcnt(0)\n"
      "s_barrier\n"
      "ds_read_b64_tr_b16 a[64:65], v6 offset:0\n"
      "ds_read_b64_tr_b16 a[66:67], v6 offset:512\n"
      "ds_read_b64_tr_b16 a[68:69], v6 offset:4096\n"
      "ds_read_b64_tr_b16 a[70:71], v6 offset:4608\n"
      "ds_read_b64_tr_b16 a[72:73], v6 offset:8192\n"
      "ds_read_b64_tr_b16 a[74:75], v6 offset:8704\n"
      "ds_read_b64_tr_b16 a[76:77], v6 offset:12288\n"
      "ds_read_b64_tr_b16 a[78:79], v6 offset:12800\n"
      "s_waitcnt lgkmcnt(0)\n"
      "s_barrier\n"
      "ds_read_b64_tr_b16 a[80:81], v6 offset:16384\n"
      "ds_read_b64_tr_b16 a[82:83], v6 offset:16896\n"
      "ds_read_b64_tr_b16 a[84:85], v6 offset:20480\n"
      "ds_read_b64_tr_b16 a[86:87], v6 offset:20992\n"
      "ds_read_b64_tr_b16 a[88:89], v6 offset:24576\n"
      "ds_read_b64_tr_b16 a[90:91], v6 offset:25088\n"
      "ds_read_b64_tr_b16 a[92:93], v6 offset:28672\n"
      "ds_read_b64_tr_b16 a[94:95], v6 offset:29184\n"
      "v_lshlrev_b32 v64, 16, v12\n"
      "v_and_b32 v65, s18, v12\n"
      "v_lshlrev_b32 v66, 16, v13\n"
      "v_and_b32 v67, s18, v13\n"
      "v_fma_f32 v48, v32, v64, v48\n"
      "v_fma_f32 v49, v33, v65, v49\n"
      "v_fma_f32 v50, v34, v66, v50\n"
      "v_fma_f32 v51, v35, v67, v51\n"
      "v_fma_f32 v52, v36, v64, v52\n"
      "v_fma_f32 v53, v37, v65, v53\n"
      "v_fma_f32 v54, v38, v66, v54\n"
      "v_fma_f32 v55, v39, v67, v55\n"
      "v_fma_f32 v56, v40, v64, v56\n"
      "v_fma_f32 v57, v41, v65, v57\n"
      "v_fma_f32 v58, v42, v66, v58\n"
      "v_fma_f32 v59, v43, v67, v59\n"
      "v_fma_f32 v60, v44, v64, v60\n"
      "v_fma_f32 v61, v45, v65, v61\n"
      "v_fma_f32 v62, v46, v66, v62\n"
      "v_fma_f32 v63, v47, v67, v63\n"
      "s_waitcnt lgkmcnt(0)\n"
      "s_barrier\n"
      "ds_read_b64_tr_b16 a[96:97], v5 offset:0\n"
      "ds_read_b64_tr_b16 a[98:99], v5 offset:512\n"
      "ds_read_b64_tr_b16 a[100:101], v5 offset:4096\n"
      "ds_read_b64_tr_b16 a[102:103], v5 offset:4608\n"
      "ds_read_b64_tr_b16 a[104:105], v5 offset:8192\n"
      "ds_read_b64_tr_b16 a[106:107], v5 offset:8704\n"
      "ds_read_b64_tr_b16 a[108:109], v5 offset:12288\n"
      "ds_read_b64_tr_b16 a[110:111], v5 offset:12800\n"
      "s_waitcnt lgkmcnt(0)\n"
      "s_barrier\n"
      "ds_read_b64_tr_b16 a[112:113], v5 offset:16384\n"
      "ds_read_b64_tr_b16 a[114:115], v5 offset:16896\n"
      "ds_read_b64_tr_b16 a[116:117], v5 offset:20480\n"
      "ds_read_b64_tr_b16 a[118:119], v5 offset:20992\n"
      "ds_read_b64_tr_b16 a[120:121], v5 offset:24576\n"
      "ds_read_b64_tr_b16 a[122:123], v5 offset:25088\n"
      "ds_read_b64_tr_b16 a[124:125], v5 offset:28672\n"
      "ds_read_b64_tr_b16 a[126:127], v5 offset:29184\n"
      "s_cmp_eq_u32 s47, 2\n"
      "s_barrier\n"
      "ds_read_b128 v[64:67], v1 offset:0\n"
      "ds_read_b128 v[68:71], v1 offset:4096\n"
      "ds_read_b128 v[72:75], v2 offset:0\n"
      "ds_read_b128 v[76:79], v2 offset:4096\n"
      "ds_read_b128 v[80:83], v3 offset:0\n"
      "ds_read_b128 v[84:87], v3 offset:4096\n"
      "ds_read_b128 v[88:91], v4 offset:0\n"
      "ds_read_b128 v[92:95], v4 offset:4096\n"
      "s_waitcnt lgkmcnt(6)\n"
      "v_mfma_f32_16x16x32_bf16 v[16:19], a[0:3], v[64:67], 0\n"
      "s_barrier\n"
      "ds_read_b128 v[96:99], v1 offset:8192\n"
      "v_mfma_f32_16x16x32_bf16 v[20:23], a[0:3], v[68:71], 0\n"
      "ds_read_b128 v[100:103], v1 offset:12288\n"
      "s_waitcnt lgkmcnt(6)\n"
      "v_mfma_f32_16x16x32_bf16 v[16:19], a[4:7], v[72:75], v[16:19]\n"
      "ds_read_b128 v[104:107], v2 offset:8192\n"
      "v_mfma_f32_16x16x32_bf16 v[20:23], a[4:7], v[76:79], v[20:23]\n"
      "ds_read_b128 v[108:111], v2 offset:12288\n"
      "s_waitcnt lgkmcnt(6)\n"
      "v_mfma_f32_16x16x32_bf16 v[16:19], a[8:11], v[80:83], v[16:19]\n"
      "ds_read_b128 v[112:115], v3 offset:8192\n"
      "v_mfma_f32_16x16x32_bf16 v[20:23], a[8:11], v[84:87], v[20:23]\n"
      "ds_read_b128 v[116:119], v3 offset:12288\n"
      "s_waitcnt lgkmcnt(6)\n"
      "v_mfma_f32_16x16x32_bf16 v[16:19], a[12:15], v[88:91], v[16:19]\n"
      "ds_read_b128 v[120:123], v4 offset:8192\n"
      "v_mfma_f32_16x16x32_bf16 v[20:23], a[12:15], v[92:95], v[20:23]\n"
      "ds_read_b128 v[124:127], v4 offset:12288\n"
      "s_waitcnt lgkmcnt(6)\n"
      "v_mfma_f32_16x16x32_bf16 v[16:19], a[16:19], v[96:99], v[16:19]\n"
      "s_barrier\n"
      "ds_read_b128 v[64:67], v1 offset:16384\n"
      "v_mfma_f32_16x16x32_bf16 v[20:23], a[16:19], v[100:103], v[20:23]\n"
      "ds_read_b128 v[68:71], v1 offset:20480\n"
      "s_waitcnt lgkmcnt(6)\n"
      "v_mfma_f32_16x16x32_bf16 v[16:19], a[20:23], v[104:107], v[16:19]\n"
      "ds_read_b128 v[72:75], v2 offset:16384\n"
      "v_mfma_f32_16x16x32_bf16 v[20:23], a[20:23], v[108:111], v[20:23]\n"
      "ds_read_b128 v[76:79], v2 offset:20480\n"
      "s_waitcnt lgkmcnt(6)\n"
      "v_mfma_f32_16x16x32_bf16 v[16:19], a[24:27], v[112:115], v[16:19]\n"
      "ds_read_b128 v[80:83], v3 offset:16384\n"
      "v_mfma_f32_16x16x32_bf16 v[20:23], a[24:27], v[116:119], v[20:23]\n"
      "ds_read_b128 v[84:87], v3 offset:20480\n"
      "s_waitcnt lgkmcnt(6)\n"
      "v_mfma_f32_16x16x32_bf16 v[16:19], a[28:31], v[120:123], v[16:19]\n"
      "ds_read_b128 v[88:91], v4 offset:16384\n"
      "v_mfma_f32_16x16x32_bf16 v[20:23], a[28:31], v[124:127], v[20:23]\n"
      "ds_read_b128 v[92:95], v4 offset:20480\n"
      "s_waitcnt lgkmcnt(6)\n"
      "v_mfma_f32_16x16x32_bf16 v[16:19], a[32:35], v[64:67], v[16:19]\n"
      "s_barrier\n"
      "ds_read_b128 v[96:99], v1 offset:24576\n"
      "v_mfma_f32_16x16x32_bf16 v[20:23], a[32:35], v[68:71], v[20:23]\n"
      "ds_read_b128 v[100:103], v1 offset:28672\n"
      "s_waitcnt lgkmcnt(6)\n"
      "v_mfma_f32_16x16x32_bf16 v[16:19], a[36:39], v[72:75], v[16:19]\n"
      "ds_read_b128 v[104:107], v2 offset:24576\n"
      "v_mfma_f32_16x16x32_bf16 v[20:23], a[36:39], v[76:79], v[20:23]\n"
      "ds_read_b128 v[108:111], v2 offset:28672\n"
      "s_waitcnt lgkmcnt(6)\n"
      "v_mfma_f32_16x16x32_bf16 v[16:19], a[40:43], v[80:83], v[16:19]\n"
      "ds_read_b128 v[112:115], v3 offset:24576\n"
      "v_mfma_f32_16x16x32_bf16 v[20:23], a[40:43], v[84:87], v[20:23]\n"
      "ds_read_b128 v[116:119], v3 offset:28672\n"
      "s_waitcnt lgkmcnt(6)\n"
      "v_mfma_f32_16x16x32_bf16 v[16:19], a[44:47], v[88:91], v[16:19]\n"
      "ds_read_b128 v[120:123], v4 offset:24576\n"
      "v_mfma_f32_16x16x32_bf16 v[20:23], a[44:47], v[92:95], v[20:23]\n"
      "ds_read_b128 v[124:127], v4 offset:28672\n"
      "s_waitcnt lgkmcnt(6)\n"
      "v_mfma_f32_16x16x32_bf16 v[16:19], a[48:51], v[96:99], v[16:19]\n"
      "s_barrier\n"
      "ds_read_b128 v[64:67], v1 offset:32768\n"
      "v_mfma_f32_16x16x32_bf16 v[20:23], a[48:51], v[100:103], v[20:23]\n"
      "ds_read_b128 v[68:71], v1 offset:36864\n"
      "s_waitcnt lgkmcnt(6)\n"
      "v_mfma_f32_16x16x32_bf16 v[16:19], a[52:55], v[104:107], v[16:19]\n"
      "ds_read_b128 v[72:75], v2 offset:32768\n"
      "v_mfma_f32_16x16x32_bf16 v[20:23], a[52:55], v[108:111], v[20:23]\n"
      "ds_read_b128 v[76:79], v2 offset:36864\n"
      "s_waitcnt lgkmcnt(6)\n"
      "v_mfma_f32_16x16x32_bf16 v[16:19], a[56:59], v[112:115], v[16:19]\n"
      "ds_read_b128 v[80:83], v3 offset:32768\n"
      "v_mfma_f32_16x16x32_bf16 v[20:23], a[56:59], v[116:119], v[20:23]\n"
      "ds_read_b128 v[84:87], v3 offset:36864\n"
      "s_waitcnt lgkmcnt(6)\n"
      "v_mfma_f32_16x16x32_bf16 v[16:19], a[60:63], v[120:123], v[16:19]\n"
      "ds_read_b128 v[88:91], v4 offset:32768\n"
      "v_mfma_f32_16x16x32_bf16 v[20:23], a[60:63], v[124:127], v[20:23]\n"
      "ds_read_b128 v[92:95], v4 offset:36864\n"
      "s_waitcnt lgkmcnt(6)\n"
      "v_mfma_f32_16x16x32_bf16 v[16:19], a[64:67], v[64:67], v[16:19]\n"
      "s_barrier\n"
      "ds_read_b128 v[96:99], v1 offset:40960\n"
      "v_mfma_f32_16x16x32_bf16 v[20:23], a[64:67], v[68:71], v[20:23]\n"
      "ds_read_b128 v[100:103], v1 offset:45056\n"
      "s_waitcnt lgkmcnt(6)\n"
      "v_mfma_f32_16x16x32_bf16 v[16:19], a[68:71], v[72:75], v[16:19]\n"
      "ds_read_b128 v[104:107], v2 offset:40960\n"
      "v_mfma_f32_16x16x32_bf16 v[20:23], a[68:71], v[76:79], v[20:23]\n"
      "ds_read_b128 v[108:111], v2 offset:45056\n"
      "s_waitcnt lgkmcnt(6)\n"
      "v_mfma_f32_16x16x32_bf16 v[16:19], a[72:75], v[80:83], v[16:19]\n"
      "ds_read_b128 v[112:115], v3 offset:40960\n"
      "v_mfma_f32_16x16x32_bf16 v[20:23], a[72:75], v[84:87], v[20:23]\n"
      "ds_read_b128 v[116:119], v3 offset:45056\n"
      "s_waitcnt lgkmcnt(6)\n"
      "v_mfma_f32_16x16x32_bf16 v[16:19], a[76:79], v[88:91], v[16:19]\n"
      "ds_read_b128 v[120:123], v4 offset:40960\n"
      "v_mfma_f32_16x16x32_bf16 v[20:23], a[76:79], v[92:95], v[20:23]\n"
      "ds_read_b128 v[124:127], v4 offset:45056\n"
      "s_waitcnt lgkmcnt(6)\n"
      "v_mfma_f32_16x16x32_bf16 v[16:19], a[80:83], v[96:99], v[16:19]\n"
      "s_barrier\n"
      "ds_read_b128 v[64:67], v1 offset:49152\n"
      "v_mfma_f32_16x16x32_bf16 v[20:23], a[80:83], v[100:103], v[20:23]\n"
      "ds_read_b128 v[68:71], v1 offset:53248\n"
      "s_waitcnt lgkmcnt(6)\n"
      "v_mfma_f32_16x16x32_bf16 v[16:19], a[84:87], v[104:107], v[16:19]\n"
      "ds_read_b128 v[72:75], v2 offset:49152\n"
      "v_mfma_f32_16x16x32_bf16 v[20:23], a[84:87], v[108:111], v[20:23]\n"
      "ds_read_b128 v[76:79], v2 offset:53248\n"
      "s_waitcnt lgkmcnt(6)\n"
      "v_mfma_f32_16x16x32_bf16 v[16:19], a[88:91], v[112:115], v[16:19]\n"
      "ds_read_b128 v[80:83], v3 offset:49152\n"
      "v_mfma_f32_16x16x32_bf16 v[20:23], a[88:91], v[116:119], v[20:23]\n"
      "ds_read_b128 v[84:87], v3 offset:53248\n"
      "s_waitcnt lgkmcnt(6)\n"
      "v_mfma_f32_16x16x32_bf16 v[16:19], a[92:95], v[120:123], v[16:19]\n"
      "ds_read_b128 v[88:91], v4 offset:49152\n"
      "v_mfma_f32_16x16x32_bf16 v[20:23], a[92:95], v[124:127], v[20:23]\n"
      "ds_read_b128 v[92:95], v4 offset:53248\n"
      "s_waitcnt lgkmcnt(6)\n"
      "v_mfma_f32_16x16x32_bf16 v[16:19], a[96:99], v[64:67], v[16:19]\n"
      "s_barrier\n"
      "ds_read_b128 v[96:99], v1 offset:57344\n"
      "v_mfma_f32_16x16x32_bf16 v[20:23], a[96:99], v[68:71], v[20:23]\n"
      "ds_read_b128 v[100:103], v1 offset:61440\n"
      "s_waitcnt lgkmcnt(6)\n"
      "v_mfma_f32_16x16x32_bf16 v[16:19], a[100:103], v[72:75], v[16:19]\n"
      "ds_read_b128 v[104:107], v2 offset:57344\n"
      "v_mfma_f32_16x16x32_bf16 v[20:23], a[100:103], v[76:79], v[20:23]\n"
      "ds_read_b128 v[108:111], v2 offset:61440\n"
      "s_waitcnt lgkmcnt(6)\n"
      "v_mfma_f32_16x16x32_bf16 v[16:19], a[104:107], v[80:83], v[16:19]\n"
      "ds_read_b128 v[112:115], v3 offset:57344\n"
      "v_mfma_f32_16x16x32_bf16 v[20:23], a[104:107], v[84:87], v[20:23]\n"
      "ds_read_b128 v[116:119], v3 offset:61440\n"
      "s_waitcnt lgkmcnt(6)\n"
      "v_mfma_f32_16x16x32_bf16 v[16:19], a[108:111], v[88:91], v[16:19]\n"
      "ds_read_b128 v[120:123], v4 offset:57344\n"
      "v_mfma_f32_16x16x32_bf16 v[20:23], a[108:111], v[92:95], v[20:23]\n"
      "ds_read_b128 v[124:127], v4 offset:61440\n"
      "s_waitcnt lgkmcnt(6)\n"
      "v_mfma_f32_16x16x32_bf16 v[16:19], a[112:115], v[96:99], v[16:19]\n"
      "s_barrier\n"
      "ds_read_b128 v[64:67], v1 offset:0\n"
      "v_mfma_f32_16x16x32_bf16 v[20:23], a[112:115], v[100:103], v[20:23]\n"
      "ds_read_b128 v[68:71], v1 offset:4096\n"
      "s_waitcnt lgkmcnt(6)\n"
      "v_mfma_f32_16x16x32_bf16 v[16:19], a[116:119], v[104:107], v[16:19]\n"
      "ds_read_b128 v[72:75], v2 offset:0\n"
      "v_mfma_f32_16x16x32_bf16 v[20:23], a[116:119], v[108:111], v[20:23]\n"
      "ds_read_b128 v[76:79], v2 offset:4096\n"
      "s_waitcnt lgkmcnt(6)\n"
      "v_mfma_f32_16x16x32_bf16 v[16:19], a[120:123], v[112:115], v[16:19]\n"
      "ds_read_b128 v[80:83], v3 offset:0\n"
      "v_mfma_f32_16x16x32_bf16 v[20:23], a[120:123], v[116:119], v[20:23]\n"
      "ds_read_b128 v[84:87], v3 offset:4096\n"
      "s_waitcnt lgkmcnt(6)\n"
      "v_mfma_f32_16x16x32_bf16 v[16:19], a[124:127], v[120:123], v[16:19]\n"
      "ds_read_b128 v[88:91], v4 offset:0\n"
      "v_mfma_f32_16x16x32_bf16 v[20:23], a[124:127], v[124:127], v[20:23]\n"
      "ds_read_b128 v[92:95], v4 offset:4096\n"
      "s_waitcnt lgkmcnt(6)\n"
      "v_mfma_f32_16x16x32_bf16 v[24:27], a[0:3], v[64:67], 0\n"
      "s_barrier\n"
      "ds_read_b128 v[96:99], v1 offset:8192\n"
      "v_mfma_f32_16x16x32_bf16 v[28:31], a[0:3], v[68:71], 0\n"
      "ds_read_b128 v[100:103], v1 offset:12288\n"
      "s_waitcnt lgkmcnt(6)\n"
      "v_mfma_f32_16x16x32_bf16 v[24:27], a[4:7], v[72:75], v[24:27]\n"
      "ds_read_b128 v[104:107], v2 offset:8192\n"
      "v_fma_f32 v16, v32, v16, v48\n"
      "v_fma_f32 v17, v33, v17, v49\n"
      "v_fma_f32 v18, v34, v18, v50\n"
      "v_mfma_f32_16x16x32_bf16 v[28:31], a[4:7], v[76:79], v[28:31]\n"
      "ds_read_b128 v[108:111], v2 offset:12288\n"
      "v_fma_f32 v19, v35, v19, v51\n"
      "v_fma_f32 v20, v36, v20, v52\n"
      "v_fma_f32 v21, v37, v21, v53\n"
      "s_waitcnt lgkmcnt(6)\n"
      "v_mfma_f32_16x16x32_bf16 v[24:27], a[8:11], v[80:83], v[24:27]\n"
      "ds_read_b128 v[112:115], v3 offset:8192\n"
      "v_fma_f32 v22, v38, v22, v54\n"
      "v_fma_f32 v23, v39, v23, v55\n"
      "v_cvt_pk_bf16_f32 v12, v16, v17\n"
      "v_mfma_f32_16x16x32_bf16 v[28:31], a[8:11], v[84:87], v[28:31]\n"
      "ds_read_b128 v[116:119], v3 offset:12288\n"
      "v_cvt_pk_bf16_f32 v14, v20, v21\n"
      "v_cvt_pk_bf16_f32 v13, v18, v19\n"
      "v_cvt_pk_bf16_f32 v15, v22, v23\n"
      "s_waitcnt lgkmcnt(6)\n"
      "v_mfma_f32_16x16x32_bf16 v[24:27], a[12:15], v[88:91], v[24:27]\n"
      "ds_read_b128 v[120:123], v4 offset:8192\n"
      "s_nop 0\n"
      "v_permlane16_swap_b32 v12, v14\n"
      "v_permlane16_swap_b32 v13, v15\n"
      "v_mfma_f32_16x16x32_bf16 v[28:31], a[12:15], v[92:95], v[28:31]\n"
      "ds_read_b128 v[124:127], v4 offset:12288\n"
      "s_cbranch_scc1 .Lsta2_%=\n"
      "buffer_store_dwordx4 v[12:15], v7, s[36:39], 0 offen sc1\n"
      ".Lsta2_%=:\n"
      "s_waitcnt lgkmcnt(6)\n"
      "v_mfma_f32_16x16x32_bf16 v[24:27], a[16:19], v[96:99], v[24:27]\n"
      "s_barrier\n"
      "ds_read_b128 v[64:67], v1 offset:16384\n"
      "v_mfma_f32_16x16x32_bf16 v[28:31], a[16:19], v[100:103], v[28:31]\n"
      "ds_read_b128 v[68:71], v1 offset:20480\n"
      "s_waitcnt lgkmcnt(6)\n"
      "v_mfma_f32_16x16x32_bf16 v[24:27], a[20:23], v[104:107], v[24:27]\n"
      "ds_read_b128 v[72:75], v2 offset:16384\n"
      "s_cbranch_scc1 .Lsta4_%=\n"
      "v_permlane16_swap_b32 v12, v14\n"
      "v_permlane16_swap_b32 v13, v15\n"
      ".Lsta4_%=:\n"
      "v_mfma_f32_16x16x32_bf16 v[28:31], a[20:23], v[108:111], v[28:31]\n"
      "ds_read_b128 v[76:79], v2 offset:20480\n"
      "s_waitcnt lgkmcnt(6)\n"
      "v_mfma_f32_16x16x32_bf16 v[24:27], a[24:27], v[112:115], v[24:27]\n"
      "ds_read_b128 v[80:83], v3 offset:16384\n"
      "v_lshlrev_b32 v48, 16, v12\n"
      "v_and_b32 v49, s18, v12\n"
      "v_mfma_f32_16x16x32_bf16 v[28:31], a[24:27], v[116:119], v[28:31]\n"
      "ds_read_b128 v[84:87], v3 offset:20480\n"
      "v_lshlrev_b32 v50, 16, v13\n"
      "v_and_b32 v51, s18, v13\n"
      "s_waitcnt lgkmcnt(6)\n"
      "v_mfma_f32_16x16x32_bf16 v[24:27], a[28:31], v[120:123], v[24:27]\n"
      "ds_read_b128 v[88:91], v4 offset:16384\n"
      "v_lshlrev_b32 v52, 16, v14\n"
      "v_and_b32 v53, s18, v14\n"
      "v_mfma_f32_16x16x32_bf16 v[28:31], a[28:31], v[124:127], v[28:31]\n"
      "ds_read_b128 v[92:95], v4 offset:20480\n"
      "v_lshlrev_b32 v54, 16, v15\n"
      "v_and_b32 v55, s18, v15\n"
      "s_waitcnt lgkmcnt(6)\n"
      "v_mfma_f32_16x16x32_bf16 v[24:27], a[32:35], v[64:67], v[24:27]\n"
      "s_barrier\n"
      "ds_read_b128 v[96:99], v1 offset:24576\n"
      "v_mfma_f32_16x16x32_bf16 v[28:31], a[32:35], v[68:71], v[28:31]\n"
      "ds_read_b128 v[100:103], v1 offset:28672\n"
      "s_waitcnt lgkmcnt(6)\n"
      "v_mfma_f32_16x16x32_bf16 v[24:27], a[36:39], v[72:75], v[24:27]\n"
      "ds_read_b128 v[104:107], v2 offset:24576\n"
      "v_mfma_f32_16x16x32_bf16 v[28:31], a[36:39], v[76:79], v[28:31]\n"
      "ds_read_b128 v[108:111], v2 offset:28672\n"
      "s_waitcnt lgkmcnt(6)\n"
      "v_mfma_f32_16x16x32_bf16 v[24:27], a[40:43], v[80:83], v[24:27]\n"
      "ds_read_b128 v[112:115], v3 offset:24576\n"
      "v_mfma_f32_16x16x32_bf16 v[28:31], a[40:43], v[84:87], v[28:31]\n"
      "ds_read_b128 v[116:119], v3 offset:28672\n"
      "s_waitcnt lgkmcnt(6)\n"
      "v_mfma_f32_16x16x32_bf16 v[24:27], a[44:47], v[88:91], v[24:27]\n"
      "ds_read_b128 v[120:123], v4 offset:24576\n"
      "v_mfma_f32_16x16x32_bf16 v[28:31], a[44:47], v[92:95], v[28:31]\n"
      "ds_read_b128 v[124:127], v4 offset:28672\n"
      "s_waitcnt lgkmcnt(6)\n"
      "v_mfma_f32_16x16x32_bf16 v[24:27], a[48:51], v[96:99], v[24:27]\n"
      "s_barrier\n"
      "ds_read_b128 v[64:67], v1 offset:32768\n"
      "v_mfma_f32_16x16x32_bf16 v[28:31], a[48:51], v[100:103], v[28:31]\n"
      "ds_read_b128 v[68:71], v1 offset:36864\n"
      "s_waitcnt lgkmcnt(6)\n"
      "v_mfma_f32_16x16x32_bf16 v[24:27], a[52:55], v[104:107], v[24:27]\n"
      "ds_read_b128 v[72:75], v2 offset:32768\n"
      "v_mfma_f32_16x16x32_bf16 v[28:31], a[52:55], v[108:111], v[28:31]\n"
      "ds_read_b128 v[76:79], v2 offset:36864\n"
      "s_cbranch_scc0 .Lsta3_%=\n"
      "buffer_store_dwordx4 v[12:15], v7, s[36:39], 0 offen nt\n"
      ".Lsta3_%=:\n"
      "s_waitcnt lgkmcnt(6)\n"
      "v_mfma_f32_16x16x32_bf16 v[24:27], a[56:59], v[112:115], v[24:27]\n"
      "ds_read_b128 v[80:83], v3 offset:32768\n"
      "v_mfma_f32_16x16x32_bf16 v[28:31], a[56:59], v[116:119], v[28:31]\n"
      "ds_read_b128 v[84:87], v3 offset:36864\n"
      "s_waitcnt lgkmcnt(6)\n"
      "v_mfma_f32_16x16x32_bf16 v[24:27], a[60:63], v[120:123], v[24:27]\n"
      "ds_read_b128 v[88:91], v4 offset:32768\n"
      "v_mfma_f32_16x16x32_bf16 v[28:31], a[60:63], v[124:127], v[28:31]\n"
      "ds_read_b128 v[92:95], v4 offset:36864\n"
      "s_waitcnt lgkmcnt(6)\n"
      "v_mfma_f32_16x16x32_bf16 v[24:27], a[64:67], v[64:67], v[24:27]\n"
      "s_barrier\n"
      "ds_read_b128 v[96:99], v1 offset:40960\n"
      "v_mfma_f32_16x16x32_bf16 v[28:31], a[64:67], v[68:71], v[28:31]\n"
      "ds_read_b128 v[100:103], v1 offset:45056\n"
      "s_waitcnt lgkmcnt(6)\n"
      "v_mfma_f32_16x16x32_bf16 v[24:27], a[68:71], v[72:75], v[24:27]\n"
      "ds_read_b128 v[104:107], v2 offset:40960\n"
      "v_mfma_f32_16x16x32_bf16 v[28:31], a[68:71], v[76:79], v[28:31]\n"
      "ds_read_b128 v[108:111], v2 offset:45056\n"
      "s_waitcnt lgkmcnt(6)\n"
      "v_mfma_f32_16x16x32_bf16 v[24:27], a[72:75], v[80:83], v[24:27]\n"
      "ds_read_b128 v[112:115], v3 offset:40960\n"
      "v_mfma_f32_16x16x32_bf16 v[28:31], a[72:75], v[84:87], v[28:31]\n"
      "ds_read_b128 v[116:119], v3 offset:45056\n"
      "s_waitcnt lgkmcnt(6)\n"
      "v_mfma_f32_16x16x32_bf16 v[24:27], a[76:79], v[88:91], v[24:27]\n"
      "ds_read_b128 v[120:123], v4 offset:40960\n"
      "v_mfma_f32_16x16x32_bf16 v[28:31], a[76:79], v[92:95], v[28:31]\n"
      "ds_read_b128 v[124:127], v4 offset:45056\n"
      "s_waitcnt lgkmcnt(6)\n"
      "v_mfma_f32_16x16x32_bf16 v[24:27], a[80:83], v[96:99], v[24:27]\n"
      "s_cbranch_scc1 .Lnowa_%=\n"
      "s_waitcnt vmcnt(0)\n"
      ".Lnowa_%=:\n"
      "s_barrier\n"
      "ds_read_b128 v[64:67], v1 offset:49152\n"
      "v_mfma_f32_16x16x32_bf16 v[28:31], a[80:83], v[100:103], v[28:31]\n"
      "ds_read_b128 v[68:71], v1 offset:53248\n"
      "s_waitcnt lgkmcnt(6)\n"
      "v_mfma_f32_16x16x32_bf16 v[24:27], a[84:87], v[104:107], v[24:27]\n"
      "ds_read_b128 v[72:75], v2 offset:49152\n"
      "v_mfma_f32_16x16x32_bf16 v[28:31], a[84:87], v[108:111], v[28:31]\n"
      "ds_read_b128 v[76:79], v2 offset:53248\n"
      "s_waitcnt lgkmcnt(6)\n"
      "v_mfma_f32_16x16x32_bf16 v[24:27], a[88:91], v[112:115], v[24:27]\n"
      "ds_read_b128 v[80:83], v3 offset:49152\n"
      "v_mfma_f32_16x16x32_bf16 v[28:31], a[88:91], v[116:119], v[28:31]\n"
      "ds_read_b128 v[84:87], v3 offset:53248\n"
      "s_waitcnt lgkmcnt(6)\n"
      "v_mfma_f32_16x16x32_bf16 v[24:27], a[92:95], v[120:123], v[24:27]\n"
      "ds_read_b128 v[88:91], v4 offset:49152\n"
      "v_mfma_f32_16x16x32_bf16 v[28:31], a[92:95], v[124:127], v[28:31]\n"
      "ds_read_b128 v[92:95], v4 offset:53248\n"
      "s_waitcnt lgkmcnt(6)\n"
      "v_mfma_f32_16x16x32_bf16 v[24:27], a[96:99], v[64:67], v[24:27]\n"
      "s_barrier\n"
      "ds_read_b128 v[96:99], v1 offset:57344\n"
      "v_mfma_f32_16x16x32_bf16 v[28:31], a[96:99], v[68:71], v[28:31]\n"
      "ds_read_b128 v[100:103], v1 offset:61440\n"
      "s_waitcnt lgkmcnt(6)\n"
      "v_mfma_f32_16x16x32_bf16 v[24:27], a[100:103], v[72:75], v[24:27]\n"
      "ds_read_b128 v[104:107], v2 offset:57344\n"
      "v_mfma_f32_16x16x32_bf16 v[28:31], a[100:103], v[76:79], v[28:31]\n"
      "ds_read_b128 v[108:111], v2 offset:61440\n"
      "s_waitcnt lgkmcnt(6)\n"
      "v_mfma_f32_16x16x32_bf16 v[24:27], a[104:107], v[80:83], v[24:27]\n"
      "ds_read_b128 v[112:115], v3 offset:57344\n"
      "v_mfma_f32_16x16x32_bf16 v[28:31], a[104:107], v[84:87], v[28:31]\n"
      "ds_read_b128 v[116:119], v3 offset:61440\n"
      "s_waitcnt lgkmcnt(6)\n"
      "v_mfma_f32_16x16x32_bf16 v[24:27], a[108:111], v[88:91], v[24:27]\n"
      "ds_read_b128 v[120:123], v4 offset:57344\n"
      "v_mfma_f32_16x16x32_bf16 v[28:31], a[108:111], v[92:95], v[28:31]\n"
      "ds_read_b128 v[124:127], v4 offset:61440\n"
      "s_waitcnt lgkmcnt(6)\n"
      "v_mfma_f32_16x16x32_bf16 v[24:27], a[112:115], v[96:99], v[24:27]\n"
      "v_mfma_f32_16x16x32_bf16 v[28:31], a[112:115], v[100:103], v[28:31]\n"
      "s_waitcnt lgkmcnt(4)\n"
      "v_mfma_f32_16x16x32_bf16 v[24:27], a[116:119], v[104:107], v[24:27]\n"
      "v_mfma_f32_16x16x32_bf16 v[28:31], a[116:119], v[108:111], v[28:31]\n"
      "s_waitcnt lgkmcnt(2)\n"
      "v_mfma_f32_16x16x32_bf16 v[24:27], a[120:123], v[112:115], v[24:27]\n"
      "v_mfma_f32_16x16x32_bf16 v[28:31], a[120:123], v[116:119], v[28:31]\n"
      "s_waitcnt lgkmcnt(0)\n"
      "v_mfma_f32_16x16x32_bf16 v[24:27], a[124:127], v[120:123], v[24:27]\n"
      "v_mfma_f32_16x16x32_bf16 v[28:31], a[124:127], v[124:127], v[28:31]\n"
      "s_nop 7\n"
      "v_fma_f32 v24, v40, v24, v56\n"
      "v_fma_f32 v25, v41, v25, v57\n"
      "v_fma_f32 v26, v42, v26, v58\n"
      "v_fma_f32 v27, v43, v27, v59\n"
      "v_fma_f32 v28, v44, v28, v60\n"
      "v_fma_f32 v29, v45, v29, v61\n"
      "v_fma_f32 v30, v46, v30, v62\n"
      "v_fma_f32 v31, v47, v31, v63\n"
      "v_cvt_pk_bf16_f32 v12, v24, v25\n"
      "v_cvt_pk_bf16_f32 v14, v28, v29\n"
      "v_cvt_pk_bf16_f32 v13, v26, v27\n"
      "v_cvt_pk_bf16_f32 v15, v30, v31\n"
      "s_nop 0\n"
      "v_permlane16_swap_b32 v12, v14\n"
      "v_permlane16_swap_b32 v13, v15\n"
      "s_cbranch_scc1 .Lcpend_%=\n"
      "buffer_store_dwordx4 v[12:15], v7, s[36:39], s51 offen sc1\n"
      "s_add_u32 s47, s47, 1\n"
      "s_cmp_eq_u32 s47, 1\n"
      "v_permlane16_swap_b32 v12, v14\n"
      "v_permlane16_swap_b32 v13, v15\n"
      "v_lshlrev_b32 v56, 16, v12\n"
      "v_and_b32 v57, s18, v12\n"
      "v_lshlrev_b32 v58, 16, v13\n"
      "v_and_b32 v59, s18, v13\n"
      "v_lshlrev_b32 v60, 16, v14\n"
      "v_and_b32 v61, s18, v14\n"
      "v_lshlrev_b32 v62, 16, v15\n"
      "v_and_b32 v63, s18, v15\n"
      "v_mov_b32 v12, v8\n"
      "v_mov_b32 v13, v9\n"
      "v_mov_b32 v8, v10\n"
      "v_mov_b32 v9, v11\n"
      "v_and_b32 v102, 63, %2\n"
      "s_cbranch_scc1 .Lcpfirst_%=\n"
      "s_lshl_b32 s14, s21, 7\n"
      "s_sub_u32 s38, 0x20000, s14\n"
      "s_add_u32 s14, s14, s17\n"
      "s_add_u32 s36, s10, s14\n"
      "s_addc_u32 s37, s11, 0\n"
      "s_mov_b32 s51, 0x10000\n"
      "v_and_b32 v100, 31, v102\n"
      "s_lshl_b32 s14, s22, 5\n"
      "v_lshl_add_u32 v100, v100, 11, s14\n"
      "v_lshrrev_b32 v101, 5, v102\n"
      "v_lshl_add_u32 v7, v101, 4, v100\n"
      "s_branch .Lcp_%=\n"
      ".Lcpfirst_%=:\n"
      "s_add_u32 s36, s36, 4194304\n"
      "s_addc_u32 s37, s37, 0\n"
      "v_lshlrev_b32 v1, 4, v102\n"
      "v_add_u32 v2, 1024, v1\n"
      "v_add_u32 v3, 2048, v1\n"
      "v_add_u32 v4, 3072, v1\n"
      "s_branch .Lcp_%=\n"
      ".Lcpend_%=:\n"
      "buffer_store_dwordx4 v[12:15], v7, s[36:39], s51 offen\n"
      ".Lend_%=:\n"
      :
      : "s"(__builtin_amdgcn_kernarg_segment_ptr()), "s"(blockIdx.x), "v"(threadIdx.x)
      :
        "memory", "vcc", "scc", "s3", "s4", "s5", "s6", "s7", "s8", "s9", "s10", "s11", "s12", "s13", "s14", "s15",
        "s16", "s17", "s18", "s19", "s20", "s21", "s22", "s23", "s24", "s25", "s26", "s27", "s28", "s29", "s30", "s31",
        "s33", "s34", "s35", "s36", "s37", "s38", "s39", "s40", "s41", "s42", "s43", "s44", "s45", "s46", "s47", "s48",
        "s49", "s50", "s51", "s52", "s53", "s54", "s55", "v1", "v2", "v3", "v4", "v5", "v6", "v7", "v8", "v9",
        "v10", "v11", "v12", "v13", "v14", "v15", "v16", "v17", "v18", "v19", "v20", "v21", "v22", "v23", "v24", "v25",
        "v26", "v27", "v28", "v29", "v30", "v31", "v32", "v33", "v34", "v35", "v36", "v37", "v38", "v39", "v40", "v41",
        "v42", "v43", "v44", "v45", "v46", "v47", "v48", "v49", "v50", "v51", "v52", "v53", "v54", "v55", "v56", "v57",
        "v58", "v59", "v60", "v61", "v62", "v63", "v64", "v65", "v66", "v67", "v68", "v69", "v70", "v71", "v72", "v73",
        "v74", "v75", "v76", "v77", "v78", "v79", "v80", "v81", "v82", "v83", "v84", "v85", "v86", "v87", "v88", "v89",
        "v90", "v91", "v92", "v93", "v94", "v95", "v96", "v97", "v98", "v99", "v100", "v101", "v102", "v103", "v104", "v105",
        "v106", "v107", "v108", "v109", "v110", "v111", "v112", "v113", "v114", "v115", "v116", "v117", "v118", "v119", "v120", "v121",
        "v122", "v123", "v124", "v125", "v126", "v127", "a0", "a1", "a2", "a3", "a4", "a5", "a6", "a7", "a8", "a9",
        "a10", "a11", "a12", "a13", "a14", "a15", "a16", "a17", "a18", "a19", "a20", "a21", "a22", "a23", "a24", "a25",
        "a26", "a27", "a28", "a29", "a30", "a31", "a32", "a33", "a34", "a35", "a36", "a37", "a38", "a39", "a40", "a41",
        "a42", "a43", "a44", "a45", "a46", "a47", "a48", "a49", "a50", "a51", "a52", "a53", "a54", "a55", "a56", "a57",
        "a58", "a59", "a60", "a61", "a62", "a63", "a64", "a65", "a66", "a67", "a68", "a69", "a70", "a71", "a72", "a73",
        "a74", "a75", "a76", "a77", "a78", "a79", "a80", "a81", "a82", "a83", "a84", "a85", "a86", "a87", "a88", "a89",
        "a90", "a91", "a92", "a93", "a94", "a95", "a96", "a97", "a98", "a99", "a100", "a101", "a102", "a103", "a104", "a105",
        "a106", "a107", "a108", "a109", "a110", "a111", "a112", "a113", "a114", "a115", "a116", "a117", "a118", "a119", "a120", "a121",
        "a122", "a123", "a124", "a125", "a126", "a127");
}

extern "C" void kernel_launch(void* const* d_in, const int* in_sizes, int n_in, void* d_out, int out_size, void* d_ws, size_t ws_size,
                              hipStream_t stream) {
  dcn_cross_kernel<<<256, 512, 0, stream>>>(d_in[0], d_in[1], d_in[2], d_out, d_ws);
}
```
